```python
import torch

def _softmax(x, axis=-1):
    return torch.softmax(x, dim=axis)
def _gelu(x):
    return torch.nn.functional.gelu(x, approximate="tanh")
def _silu(x):
    return torch.nn.functional.silu(x)
def _sigmoid(x):
    return torch.sigmoid(x)
def _layernorm(x, axis=-1, eps=1e-5):
    m = x.mean(dim=axis, keepdim=True)
    v = x.var(dim=axis, keepdim=True, unbiased=False)
    return (x - m) / torch.sqrt(v + eps)
def _scatter_add(tgt, idx, src, axis=0):
    return tgt.clone().index_add_(axis, idx, src)
def _segment_sum(src, seg, n):
    out = torch.zeros((n,) + src.shape[1:], dtype=src.dtype)
    return out.index_add_(0, seg, src)
def _pad(x, pw):
    flat = tuple(v for pair in reversed(pw) for v in pair)
    return torch.nn.functional.pad(x, flat)

kernel_name = 'gated_mix__mha_block_cached__glu_gated_swiglu__decode'

def setup_inputs():

    _w = (lambda a: (lambda t: t.to(torch.bfloat16) if t.is_floating_point() else t)(torch.as_tensor(a)))
    def _A_setup():
        B, Sq, D = 4, 1, 640
        H, Sk, Dh = 32, 8192, 192
        return {
            "x": _w((lambda *s: torch.randn(*s, dtype=torch.bfloat16) * 0.05)(B, Sq, D)),
            "k_cache": _w((lambda *s: torch.randn(*s, dtype=torch.bfloat16) * 6.4)(B, H, Sk, Dh)),
            "v_cache": _w((lambda *s: torch.randn(*s, dtype=torch.bfloat16) * 0.05)(B, H, Sk, Dh)),
            "wq": _w((lambda *s: torch.randn(*s, dtype=torch.bfloat16) * 6.4)(D, H * Dh) * (D ** -0.5)),
            "wo": _w((lambda *s: torch.randn(*s, dtype=torch.bfloat16) * 0.05)(H * Dh, D) * ((H * Dh) ** -0.5)),
        }
    _A_in = _A_setup()

    def _B_setup():
        B, S, D, F = 4, 1, 640, 1536
        return {
            "x": _w((lambda *s: torch.randn(*s, dtype=torch.bfloat16) * 0.05)(B, S, D)),
            "w1": _w((lambda *s: torch.randn(*s, dtype=torch.bfloat16) * 0.4)(D, F) * (2.0 / D) ** 0.5),
            "w3": _w((lambda *s: torch.randn(*s, dtype=torch.bfloat16) * 0.4)(D, F) * (2.0 / D) ** 0.5),
            "w2": _w((lambda *s: torch.randn(*s, dtype=torch.bfloat16) * 0.4)(F, D) * (2.0 / F) ** 0.5),
        }
    _B_in = _B_setup()

    return {
        "A_x": _A_in["x"],
        "A_k_cache": _A_in["k_cache"],
        "A_v_cache": _A_in["v_cache"],
        "A_wq": _A_in["wq"],
        "A_wo": _A_in["wo"],
        "B_x": _B_in["x"],
        "B_w1": _B_in["w1"],
        "B_w3": _B_in["w3"],
        "B_w2": _B_in["w2"],
        "gate_w": _w((lambda *s: torch.randn(*s, dtype=torch.bfloat16) * 0.05)(640) * 16.0),
    }

def reference(A_x, A_k_cache, A_v_cache, A_wq, A_wo, B_x, B_w1, B_w3, B_w2, gate_w):
    def _A_ref(x, k_cache, v_cache, wq, wo):
        B, Sq, D = x.shape
        H, Dh = 32, 192
        q = (lambda x, *d: x.permute(*d))((x @ wq).reshape(B, Sq, H, Dh), 0, 2, 1, 3)
        scores = torch.einsum("bhqd,bhkd->bhqk", q, k_cache) * (Dh ** -0.5)
        p = _softmax(scores, axis=-1)
        o = torch.einsum("bhqk,bhkd->bhqd", p, v_cache)
        return (lambda x, *d: x.permute(*d))(o, 0, 2, 1, 3).reshape(B, Sq, H * Dh) @ wo

    def _B_ref(x, w1, w3, w2):
        gate = torch.einsum("bsd,df->bsf", x, w1)
        up = torch.einsum("bsd,df->bsf", x, w3)
        h = _silu(gate) * up
        out = torch.einsum("bsf,fd->bsd", h, w2)
        return out

    x = A_x
    g = _sigmoid((x * gate_w).sum(-1, keepdims=True))
    a = _A_ref(x, A_k_cache, A_v_cache, A_wq, A_wo)
    b = _B_ref(x, B_w1, B_w3, B_w2)
    return g * a + (1 - g) * b
```

```cpp
#include <hip/hip_runtime.h>
#include <hip/hip_bf16.h>
#include <stdint.h>

constexpr int B = 4, D = 640, H = 32, SK = 8192, DH = 192, F = 1536;
constexpr int NSPLIT = 4;
constexpr int KEYS = SK / NSPLIT;

typedef unsigned u32x4 __attribute__((ext_vector_type(4)));
typedef float f32x4 __attribute__((ext_vector_type(4)));
using bf16x2 = __attribute__((ext_vector_type(2))) __bf16;

__device__ __forceinline__ float bf2f(unsigned short b) { return __uint_as_float((unsigned)b << 16); }
__device__ __forceinline__ float bflo(unsigned u) { return __uint_as_float(u << 16); }
__device__ __forceinline__ float bfhi(unsigned u) { return __uint_as_float(u & 0xffff0000u); }
__device__ __forceinline__ unsigned short f2bf(float f) {
  unsigned u = __float_as_uint(f);
  u += 0x7FFFu + ((u >> 16) & 1u);
  return (unsigned short)(u >> 16);
}
__device__ __forceinline__ float rbf(float f) { return bf2f(f2bf(f)); }
__device__ __forceinline__ float dot2(unsigned k, unsigned q, float acc) {
  return __builtin_amdgcn_fdot2_f32_bf16(__builtin_bit_cast(bf16x2, k), __builtin_bit_cast(bf16x2, q), acc, false);
}
template <int CTRL> __device__ __forceinline__ float dpp(float x) {
  return __builtin_bit_cast(float, __builtin_amdgcn_mov_dpp(__builtin_bit_cast(int, x), CTRL, 0xf, 0xf, true));
}

constexpr size_t WS_Q = 0;
constexpr size_t WS_H = 65536;
constexpr size_t WS_PO = 131072;
constexpr size_t WS_PML = WS_PO + (size_t)128 * NSPLIT * DH * 4;

__global__ void __launch_bounds__(256) k_pre(const unsigned short* x, const unsigned short* wq, const unsigned short* w1,
                                              const unsigned short* w3, unsigned char* ws) {
  __shared__ float xs[B][D];
  __shared__ float red[4][B][64];
  __shared__ float red2[4][B][64];
  for (int i = threadIdx.x; i < B * D; i += 256) xs[i / D][i % D] = bf2f(x[i]);
  __syncthreads();
  const int col = threadIdx.x & 63, part = threadIdx.x >> 6;
  const int blk = blockIdx.x;
  if (blk < 96) {
    const int n = blk * 64 + col;
    float acc[B] = {0, 0, 0, 0};
    for (int k = part * 160; k < part * 160 + 160; ++k) {
      const float w = bf2f(wq[(size_t)k * (H * DH) + n]);
      for (int b = 0; b < B; ++b) acc[b] = fmaf(xs[b][k], w, acc[b]);
    }
    for (int b = 0; b < B; ++b) red[part][b][col] = acc[b];
    __syncthreads();
    if (part == 0) {
      unsigned short* q = (unsigned short*)(ws + WS_Q);
      for (int b = 0; b < B; ++b) q[b * (H * DH) + n] = f2bf(red[0][b][col] + red[1][b][col] + red[2][b][col] + red[3][b][col]);
    }
  } else {
    const int n = (blk - 96) * 64 + col;
    float a1[B] = {0, 0, 0, 0}, a3[B] = {0, 0, 0, 0};
    for (int k = part * 160; k < part * 160 + 160; ++k) {
      const float wa = bf2f(w1[(size_t)k * F + n]), wb = bf2f(w3[(size_t)k * F + n]);
      for (int b = 0; b < B; ++b) { a1[b] = fmaf(xs[b][k], wa, a1[b]); a3[b] = fmaf(xs[b][k], wb, a3[b]); }
    }
    for (int b = 0; b < B; ++b) { red[part][b][col] = a1[b]; red2[part][b][col] = a3[b]; }
    __syncthreads();
    if (part == 0) {
      float* h = (float*)(ws + WS_H);
      for (int b = 0; b < B; ++b) {
        const float g = rbf(red[0][b][col] + red[1][b][col] + red[2][b][col] + red[3][b][col]);
        const float u = rbf(red2[0][b][col] + red2[1][b][col] + red2[2][b][col] + red2[3][b][col]);
        const float s = rbf(g / (1.f + __expf(-g)));
        h[b * F + n] = rbf(s * u);
      }
    }
  }
}

__global__ void __launch_bounds__(256) k_attn(const unsigned short* kc, const unsigned short* vc, unsigned char* ws) {
  __shared__ float mrg[4][8][28];
  __shared__ float mw[4][8][2];
  const int bh = blockIdx.x / NSPLIT, sp = blockIdx.x % NSPLIT;
  const int b = bh / H, h = bh % H;
  const int lane = threadIdx.x & 63, wave = threadIdx.x >> 6;
  const int r = lane >> 3, c = lane & 7;
  const unsigned short* q = (const unsigned short*)(ws + WS_Q) + b * (H * DH) + h * DH;
  u32x4 qv[3];
  for (int i = 0; i < 3; ++i) qv[i] = *(const u32x4*)(q + (c + 8 * i) * 8);
  const size_t base = ((size_t)bh * SK + (size_t)sp * KEYS) * DH;
  const float C2 = 0.07216878364870322f * 1.4426950408889634f;
  float m = -INFINITY, l = 0.f, acc[24];
  for (int i = 0; i < 24; ++i) acc[i] = 0.f;
  constexpr int T = 2;
  for (int k0 = wave * 8 * T; k0 < KEYS; k0 += 4 * 8 * T) {
    u32x4 kv[T][3], vv[T][3];
    for (int t = 0; t < T; ++t)
      for (int i = 0; i < 3; ++i) {
        const size_t off = base + (size_t)(k0 + t * 8 + r) * DH + (c + 8 * i) * 8;
        kv[t][i] = __builtin_nontemporal_load((const u32x4*)(kc + off));
        vv[t][i] = __builtin_nontemporal_load((const u32x4*)(vc + off));
      }
    float s[T];
    float mx = m;
    for (int t = 0; t < T; ++t) {
      float d = 0.f;
      for (int i = 0; i < 3; ++i)
        for (int j = 0; j < 4; ++j) d = dot2(kv[t][i][j], qv[i][j], d);
      d += dpp<0xB1>(d);
      d += dpp<0x4E>(d);
      d += dpp<0x141>(d);
      s[t] = d * C2;
      mx = fmaxf(mx, s[t]);
    }
    const float alpha = __builtin_amdgcn_exp2f(m - mx);
    m = mx;
    l *= alpha;
    for (int i = 0; i < 24; ++i) acc[i] *= alpha;
    for (int t = 0; t < T; ++t) {
      const float p = __builtin_amdgcn_exp2f(s[t] - m);
      l += p;
      for (int i = 0; i < 3; ++i)
        for (int j = 0; j < 4; ++j) {
          acc[i * 8 + 2 * j] = fmaf(p, bflo(vv[t][i][j]), acc[i * 8 + 2 * j]);
          acc[i * 8 + 2 * j + 1] = fmaf(p, bfhi(vv[t][i][j]), acc[i * 8 + 2 * j + 1]);
        }
    }
  }
  __shared__ float big[32][DH];
  const int g = wave * 8 + r;
  for (int i = 0; i < 3; ++i)
    for (int j = 0; j < 8; ++j) big[g][(c + 8 * i) * 8 + j] = acc[i * 8 + j];
  if (c == 0) { mw[wave][r][0] = m; mw[wave][r][1] = l; }
  __syncthreads();
  if (threadIdx.x < DH) {
    float M = -INFINITY;
    for (int j = 0; j < 32; ++j) M = fmaxf(M, mw[j >> 3][j & 7][0]);
    float L = 0.f, o = 0.f;
    for (int j = 0; j < 32; ++j) {
      const float w = __builtin_amdgcn_exp2f(mw[j >> 3][j & 7][0] - M);
      L = fmaf(w, mw[j >> 3][j & 7][1], L);
      o = fmaf(w, big[j][threadIdx.x], o);
    }
    float* po = (float*)(ws + WS_PO) + (size_t)blockIdx.x * DH;
    float* pml = (float*)(ws + WS_PML) + (size_t)blockIdx.x * 2;
    po[threadIdx.x] = o;
    if (threadIdx.x == 0) { pml[0] = M; pml[1] = L; }
  }
  (void)mrg;
}

__global__ void __launch_bounds__(256) k_post(const unsigned short* x, const unsigned short* wo, const unsigned short* w2,
                                               const unsigned short* gw, const unsigned char* ws, unsigned short* out) {
  __shared__ float o[B][H * DH];
  __shared__ float red[16][B][16];
  __shared__ float red2[16][B][16];
  __shared__ float gs[B];
  const float* po = (const float*)(ws + WS_PO);
  const float* pml = (const float*)(ws + WS_PML);
  for (int i = threadIdx.x; i < B * H * DH; i += 256) {
    const int bh = i / DH, d = i % DH;
    float M = -INFINITY;
    for (int s = 0; s < NSPLIT; ++s) M = fmaxf(M, pml[(bh * NSPLIT + s) * 2]);
    float L = 0.f, a = 0.f;
    for (int s = 0; s < NSPLIT; ++s) {
      const float w = __builtin_amdgcn_exp2f(pml[(bh * NSPLIT + s) * 2] - M);
      L = fmaf(w, pml[(bh * NSPLIT + s) * 2 + 1], L);
      a = fmaf(w, po[(size_t)(bh * NSPLIT + s) * DH + d], a);
    }
    o[bh / H][(bh % H) * DH + d] = rbf(a / L);
  }
  if (threadIdx.x < 64 * B) {
    const int b = threadIdx.x >> 6, lane = threadIdx.x & 63;
    float s = 0.f;
    for (int k = lane; k < D; k += 64) s += rbf(bf2f(x[b * D + k]) * bf2f(gw[k]));
    for (int off = 32; off; off >>= 1) s += __shfl_xor(s, off);
    if (lane == 0) gs[b] = rbf(1.f / (1.f + __expf(-rbf(s))));
  }
  __syncthreads();
  const int col = threadIdx.x & 15, part = threadIdx.x >> 4;
  const int n = blockIdx.x * 16 + col;
  float a[B] = {0, 0, 0, 0}, f[B] = {0, 0, 0, 0};
  for (int k = part; k < H * DH; k += 16) {
    const float w = bf2f(wo[(size_t)k * D + n]);
    for (int b = 0; b < B; ++b) a[b] = fmaf(o[b][k], w, a[b]);
  }
  const float* h = (const float*)(ws + WS_H);
  for (int k = part; k < F; k += 16) {
    const float w = bf2f(w2[(size_t)k * D + n]);
    for (int b = 0; b < B; ++b) f[b] = fmaf(h[b * F + k], w, f[b]);
  }
  for (int b = 0; b < B; ++b) { red[part][b][col] = a[b]; red2[part][b][col] = f[b]; }
  __syncthreads();
  if (threadIdx.x < 64) {
    const int b = threadIdx.x >> 4, cc = threadIdx.x & 15;
    float sa = 0.f, sf = 0.f;
    for (int p = 0; p < 16; ++p) { sa += red[p][b][cc]; sf += red2[p][b][cc]; }
    const float g = gs[b];
    const float ra = rbf(g * rbf(sa)), rb = rbf(rbf(1.f - g) * rbf(sf));
    out[b * D + blockIdx.x * 16 + cc] = f2bf(ra + rb);
  }
}

extern "C" void kernel_launch(void* const* d_in, const int* in_sizes, int n_in, void* d_out, int out_size, void* d_ws,
                              size_t ws_size, hipStream_t stream) {
  const unsigned short* x = (const unsigned short*)d_in[0];
  const unsigned short* kc = (const unsigned short*)d_in[1];
  const unsigned short* vc = (const unsigned short*)d_in[2];
  const unsigned short* wq = (const unsigned short*)d_in[3];
  const unsigned short* wo = (const unsigned short*)d_in[4];
  const unsigned short* w1 = (const unsigned short*)d_in[6];
  const unsigned short* w3 = (const unsigned short*)d_in[7];
  const unsigned short* w2 = (const unsigned short*)d_in[8];
  const unsigned short* gw = (const unsigned short*)d_in[9];
  unsigned char* ws = (unsigned char*)d_ws;
  k_pre<<<120, 256, 0, stream>>>(x, wq, w1, w3, ws);
  k_attn<<<128 * NSPLIT, 256, 0, stream>>>(kc, vc, ws);
  k_post<<<40, 256, 0, stream>>>(x, wo, w2, gw, ws, (unsigned short*)d_out);
}
```

```cpp
#include <hip/hip_runtime.h>
#include <stdint.h>

namespace {

typedef unsigned u32x4 __attribute__((ext_vector_type(4)));
typedef unsigned u32x3 __attribute__((ext_vector_type(3)));
typedef unsigned u32x2 __attribute__((ext_vector_type(2)));
typedef float f32x4 __attribute__((ext_vector_type(4)));
typedef __bf16 bf16x2 __attribute__((ext_vector_type(2)));
typedef __bf16 bf16x8 __attribute__((ext_vector_type(8)));
#define LAS __attribute__((address_space(3)))

constexpr int DM = 640, NH = 32, SK = 8192, DH = 192, FF = 1536;
constexpr int NTHR = 512;
constexpr unsigned KV_WG_BYTES = 4096u * 384u;
constexpr int AUX_NT = 2, AUX_SC1 = 16;
constexpr int RSRC_W3 = 0x00020000;

constexpr int WS_E = 0;
constexpr int WS_ML = 4096;
constexpr int WS_HB = WS_ML + 128 * 16 * 16;
constexpr int WS_O = WS_HB + 8 * 4 * 96 * 8;
constexpr int WS_Y = WS_O + 32 * 4 * 192 * 16;
constexpr int WS_YF = WS_Y + 8 * 4 * 4 * 32 * 160;
constexpr int WS_END = WS_YF + 8 * 4 * 4 * 8 * 160;

constexpr int L_SC = 0;
constexpr int L_RED = L_SC + 8 * 512 * 4;
constexpr int L_FRED = L_RED + 8 * 192 * 4;
constexpr int L_QB = L_FRED + 8 * 48 * 4;
constexpr int L_OB = L_QB + 384;
constexpr int L_MISC = L_OB + 4 * 384;
constexpr int L_WO = L_MISC + 64;
constexpr int L_W2 = L_WO + 192 * 160;
constexpr int L_END = L_W2 + 192 * 160;
static_assert(L_END > 80 * 1024 && L_END <= 160 * 1024, "one workgroup per CU");

constexpr unsigned SPIN_CAP = 1u << 22;

__device__ __forceinline__ float bflo(unsigned u) { return __uint_as_float(u << 16); }
__device__ __forceinline__ float bfhi(unsigned u) { return __uint_as_float(u & 0xffff0000u); }
__device__ __forceinline__ unsigned cvtpk(float lo, float hi) {
  bf16x2 r = {(__bf16)lo, (__bf16)hi};
  return __builtin_bit_cast(unsigned, r);
}
__device__ __forceinline__ float rbf(float f) { return bflo(cvtpk(f, f) & 0xffffu); }
__device__ __forceinline__ float dot2(unsigned a, unsigned b, float c) {
  return __builtin_amdgcn_fdot2_f32_bf16(__builtin_bit_cast(bf16x2, a), __builtin_bit_cast(bf16x2, b), c, false);
}
template <int CTRL> __device__ __forceinline__ float dpp(float x) {
  return __builtin_bit_cast(float, __builtin_amdgcn_mov_dpp(__builtin_bit_cast(int, x), CTRL, 0xf, 0xf, true));
}
constexpr int XOR1 = 0xB1, XOR2 = 0x4E, HMIR = 0x141, MIR = 0x140, ROR8 = 0x128;
__device__ __forceinline__ float swap32_add(float a, float b) {
  auto s = __builtin_amdgcn_permlane32_swap(__float_as_uint(a), __float_as_uint(b), false, false);
  return __uint_as_float(s[0]) + __uint_as_float(s[1]);
}
__device__ __forceinline__ float swap16_add(float a, float b) {
  auto s = __builtin_amdgcn_permlane16_swap(__float_as_uint(a), __float_as_uint(b), false, false);
  return __uint_as_float(s[0]) + __uint_as_float(s[1]);
}
__device__ __forceinline__ float swap32_max(float a) {
  auto s = __builtin_amdgcn_permlane32_swap(__float_as_uint(a), __float_as_uint(a), false, false);
  return fmaxf(__uint_as_float(s[0]), __uint_as_float(s[1]));
}
__device__ __forceinline__ float swap16_max(float a) {
  auto s = __builtin_amdgcn_permlane16_swap(__float_as_uint(a), __float_as_uint(a), false, false);
  return fmaxf(__uint_as_float(s[0]), __uint_as_float(s[1]));
}
__device__ __forceinline__ float row16_sum(float x) { x += dpp<XOR1>(x); x += dpp<XOR2>(x); x += dpp<HMIR>(x); x += dpp<MIR>(x); return x; }
__device__ __forceinline__ float row16_max(float x) {
  x = fmaxf(x, dpp<XOR1>(x)); x = fmaxf(x, dpp<XOR2>(x)); x = fmaxf(x, dpp<HMIR>(x)); x = fmaxf(x, dpp<MIR>(x)); return x;
}
__device__ __forceinline__ float wave_sum(float x) { x = row16_sum(x); x = swap16_add(x, x); return swap32_add(x, x); }
__device__ __forceinline__ float wave_max(float x) { x = row16_max(x); x = swap16_max(x); return swap32_max(x); }

typedef __amdgpu_buffer_rsrc_t rsrc_t;
__device__ __forceinline__ rsrc_t mk(const void* p, unsigned bytes) { return __builtin_amdgcn_make_buffer_rsrc((void*)p, 0, bytes, RSRC_W3); }
template <int AUX> __device__ __forceinline__ u32x4 ld4(rsrc_t r, int voff, int soff) { return __builtin_amdgcn_raw_buffer_load_b128(r, voff, soff, AUX); }

__device__ __forceinline__ void reduce24(const float (&a)[24], float (&o)[3], int lane) {
  float t[12], u[6];
#pragma unroll
  for (int i = 0; i < 12; ++i) t[i] = swap32_add(a[i], a[i + 12]);
#pragma unroll
  for (int i = 0; i < 6; ++i) u[i] = swap16_add(t[i], t[i + 6]);
#pragma unroll
  for (int i = 0; i < 3; ++i) {
    const float x = u[i] + dpp<ROR8>(u[i]), y = u[i + 3] + dpp<ROR8>(u[i + 3]);
    o[i] = (lane & 8) ? y : x;
  }
}

__device__ __forceinline__ void stage_slice(const unsigned short* src, int dst, int wave, int lane) {
#pragma unroll
  for (int u = 0; u < 4; ++u) {
    const int blk = u * 8 + wave;
    if (blk < 30) {
      const int id = blk * 64 + lane;
      const unsigned short* g = src + (id / 10) * DM + (id % 10) * 8;
      unsigned keep;
      asm volatile("s_mov_b32 %0, m0\n\ts_mov_b32 m0, %2\n\ts_nop 0\n\tglobal_load_lds_dwordx4 %1, off\n\ts_mov_b32 m0, %0"
                   : "=&s"(keep) : "v"(g), "s"(dst + blk * 1024) : "memory");
    }
  }
}

struct Params {
  const unsigned short* x;
  const unsigned short* kc;
  const unsigned short* vc;
  const unsigned short* wq;
  const unsigned short* wo;
  const unsigned short* w1;
  const unsigned short* w3;
  const unsigned short* w2;
  const unsigned short* gw;
  unsigned short* out;
  unsigned char* ws;
};

#define STAMP(i) do { } while (0)
#define STAMP_DECL do { } while (0)
#define STAMP_FLUSH do { } while (0)

__global__ void __launch_bounds__(NTHR, 2) fused_decode(Params p) {
  extern __shared__ __attribute__((aligned(16))) unsigned char lds_raw[];
  LAS unsigned char* L = (LAS unsigned char*)lds_raw;
  const int tid = threadIdx.x, lane = tid & 63;
  const int wave = __builtin_amdgcn_readfirstlane(tid >> 6);
  const int r = lane >> 3, c = lane & 7;
  const int w = blockIdx.x, xcd = w & 7, i32 = w >> 3;
  const int h = xcd * 4 + (i32 & 3), bs = i32 >> 2, b = bs >> 1, half = bs & 1;
  const bool ffn_wg = (i32 & 3) == 0;
  STAMP_DECL;
  STAMP(0);

  const rsrc_t rws = mk(p.ws, WS_END);
  const size_t kvbase = ((size_t)(b * NH + h) * SK + (size_t)half * 4096) * DH;
  const rsrc_t rk = mk(p.kc + kvbase, KV_WG_BYTES);
  const rsrc_t rv = mk(p.vc + kvbase, KV_WG_BYTES);
  const int voff = r * 384 + c * 16;
  const int tile0 = wave * 3072;

  u32x4 ring[4][3];
#pragma unroll
  for (int u = 0; u < 4; ++u)
#pragma unroll
    for (int i = 0; i < 3; ++i) ring[u][i] = ld4<AUX_NT>(rk, voff + i * 128, tile0 + u * 24576);
  __builtin_amdgcn_sched_barrier(0);

  const unsigned epoch_in = __builtin_amdgcn_raw_buffer_load_b32(rws, WS_E, 0, AUX_SC1);
  if (ffn_wg) stage_slice(p.w2 + (size_t)xcd * DH * DM + bs * 80, L_W2, wave, lane);
  const rsrc_t rx = mk(p.x, 4 * DM * 2);
  const int xo = (b * DM + 80 * wave + 10 * r) * 2;
  const u32x4 xq4 = ld4<0>(rx, xo, 0);
  const unsigned xq1 = __builtin_amdgcn_raw_buffer_load_b32(rx, xo + 16, 0, 0);
  const int f0 = xcd * 192 + i32 * 6;
  const int k1 = 80 * wave + lane;
  const int oob = 0x40000000;
  const int fo1 = (k1 * FF + f0) * 2, fo2 = lane < 16 ? fo1 + 64 * FF * 2 : oob;
  const rsrc_t r1 = mk(p.w1, DM * FF * 2), r3 = mk(p.w3, DM * FF * 2);
  u32x3 wa[2], wb[2];
  wa[0] = __builtin_amdgcn_raw_buffer_load_b96(r1, fo1, 0, 0);
  wb[0] = __builtin_amdgcn_raw_buffer_load_b96(r3, fo1, 0, 0);
  wa[1] = __builtin_amdgcn_raw_buffer_load_b96(r1, fo2, 0, 0);
  wb[1] = __builtin_amdgcn_raw_buffer_load_b96(r3, fo2, 0, 0);
  unsigned xf[2][4];
#pragma unroll
  for (int bb = 0; bb < 4; ++bb) {
    xf[0][bb] = __builtin_amdgcn_raw_buffer_load_b16(rx, (bb * DM + k1) * 2, 0, 0);
    xf[1][bb] = __builtin_amdgcn_raw_buffer_load_b16(rx, lane < 16 ? (bb * DM + k1 + 64) * 2 : oob, 0, 0);
  }
  const rsrc_t rq = mk(p.wq + h * DH, (DM * NH * DH - h * DH) * 2);
  const int qo = r * (10 * NH * DH * 2) + c * 16;
  u32x4 wqv[10][3];
#pragma unroll
  for (int t = 0; t < 10; ++t)
#pragma unroll
    for (int i = 0; i < 3; ++i) wqv[t][i] = ld4<0>(rq, qo + i * 128, (80 * wave + t) * (NH * DH * 2));
  __builtin_amdgcn_sched_barrier(0);

  STAMP(1);
  {
    float a[24];
#pragma unroll
    for (int i = 0; i < 24; ++i) a[i] = 0.f;
#pragma unroll
    for (int t = 0; t < 10; ++t) {
      const unsigned xw = t < 8 ? xq4[t >> 1] : xq1;
      const unsigned xl = (t & 1) ? (xw >> 16) : (xw & 0xffffu), xh = (t & 1) ? (xw & 0xffff0000u) : (xw << 16);
#pragma unroll
      for (int i = 0; i < 3; ++i)
#pragma unroll
        for (int j = 0; j < 4; ++j) {
          a[i * 8 + 2 * j] = dot2(wqv[t][i][j], xl, a[i * 8 + 2 * j]);
          a[i * 8 + 2 * j + 1] = dot2(wqv[t][i][j], xh, a[i * 8 + 2 * j + 1]);
        }
    }
    float o[3];
    reduce24(a, o, lane);
    const int base = 12 * (lane >> 5) + 6 * ((lane >> 4) & 1) + 3 * ((lane >> 3) & 1);
    LAS float* red = (LAS float*)(L + L_RED) + wave * 192;
#pragma unroll
    for (int i = 0; i < 3; ++i) { const int ai = base + i; red[(c + 8 * (ai >> 3)) * 8 + (ai & 7)] = o[i]; }
  }
  __builtin_amdgcn_sched_barrier(0);
  STAMP(2);
  stage_slice(p.wo + (size_t)h * DH * DM + bs * 80, L_WO, wave, lane);
  __builtin_amdgcn_sched_barrier(0);
  {
    float g[24], u_[24];
#pragma unroll
    for (int i = 0; i < 24; ++i) { g[i] = 0.f; u_[i] = 0.f; }
#pragma unroll
    for (int rr = 0; rr < 2; ++rr)
#pragma unroll
      for (int bb = 0; bb < 4; ++bb) {
        const unsigned xl = xf[rr][bb] & 0xffffu, xh = xl << 16;
#pragma unroll
        for (int j = 0; j < 3; ++j) {
          g[bb * 6 + 2 * j] = dot2(wa[rr][j], xl, g[bb * 6 + 2 * j]);
          g[bb * 6 + 2 * j + 1] = dot2(wa[rr][j], xh, g[bb * 6 + 2 * j + 1]);
          u_[bb * 6 + 2 * j] = dot2(wb[rr][j], xl, u_[bb * 6 + 2 * j]);
          u_[bb * 6 + 2 * j + 1] = dot2(wb[rr][j], xh, u_[bb * 6 + 2 * j + 1]);
        }
      }
    float t[24], s12[12], s6[6], s3[3];
#pragma unroll
    for (int i = 0; i < 24; ++i) t[i] = swap32_add(g[i], u_[i]);
#pragma unroll
    for (int i = 0; i < 12; ++i) s12[i] = swap16_add(t[i], t[i + 12]);
#pragma unroll
    for (int i = 0; i < 6; ++i) {
      const float x = s12[i] + dpp<ROR8>(s12[i]), y = s12[i + 6] + dpp<ROR8>(s12[i + 6]);
      s6[i] = (lane & 8) ? y : x;
    }
#pragma unroll
    for (int i = 0; i < 3; ++i) {
      float x = s6[i], y = s6[i + 3];
      x += dpp<XOR1>(x); x += dpp<XOR2>(x); x += dpp<HMIR>(x);
      y += dpp<XOR1>(y); y += dpp<XOR2>(y); y += dpp<HMIR>(y);
      s3[i] = (lane & 4) ? y : x;
    }
    if ((lane & 3) == 0) {
      const int idx = 24 * (lane >> 5) + 12 * ((lane >> 4) & 1) + 6 * ((lane >> 3) & 1) + 3 * ((lane >> 2) & 1);
      LAS float* fr = (LAS float*)(L + L_FRED) + wave * 48 + idx;
      fr[0] = s3[0]; fr[1] = s3[1]; fr[2] = s3[2];
    }
  }
  STAMP(3);
  if (tid == 0) { ((LAS unsigned*)(L + L_MISC))[0] = epoch_in + 1u; ((LAS unsigned*)(L + L_MISC))[1] = 0u; }
  __syncthreads();
  STAMP(4);
  const unsigned tag = __builtin_amdgcn_readfirstlane(((LAS unsigned*)(L + L_MISC))[0]);
  if (tid < 192) {
    const LAS float* red = (const LAS float*)(L + L_RED);
    float s = 0.f;
#pragma unroll
    for (int v = 0; v < 8; ++v) s += red[v * 192 + tid];
    ((LAS unsigned short*)(L + L_QB))[tid] = (unsigned short)(cvtpk(s, s) & 0xffffu);
  } else if (tid >= 256 && tid < 256 + 12) {
    const int i = tid - 256, bb = i / 3, jp = i % 3;
    const LAS float* fr = (const LAS float*)(L + L_FRED);
    float hv[2];
#pragma unroll
    for (int e = 0; e < 2; ++e) {
      float g = 0.f, u = 0.f;
#pragma unroll
      for (int v = 0; v < 8; ++v) { g += fr[v * 48 + bb * 6 + 2 * jp + e]; u += fr[v * 48 + 24 + bb * 6 + 2 * jp + e]; }
      g = rbf(g); u = rbf(u);
      const float s = rbf(g / (1.f + __expf(-g)));
      hv[e] = s * u;
    }
    const u32x2 gr = {cvtpk(hv[0], hv[1]), tag};
    __builtin_amdgcn_raw_buffer_store_b64(gr, rws, WS_HB + ((xcd * 4 + bb) * 96 + i32 * 3 + jp) * 8, 0, AUX_SC1);
  }
  STAMP(5);
  __syncthreads();
  u32x4 qv[3];
#pragma unroll
  for (int i = 0; i < 3; ++i) qv[i] = *(const LAS u32x4*)(L + L_QB + (c + 8 * i) * 16);
  STAMP(6);

  LAS float* sc = (LAS float*)(L + L_SC) + wave * 512;
  float mx = -INFINITY;
  for (int it = 0; it < 8; ++it) {
    float sreg = 0.f;
#pragma unroll
    for (int u = 0; u < 8; ++u) {
      const int slot = u & 3;
      float d0 = 0.f, d1 = 0.f, d2 = 0.f;
#pragma unroll
      for (int j = 0; j < 4; ++j) {
        d0 = dot2(ring[slot][0][j], qv[0][j], d0);
        d1 = dot2(ring[slot][1][j], qv[1][j], d1);
        d2 = dot2(ring[slot][2][j], qv[2][j], d2);
      }
      __builtin_amdgcn_sched_barrier(0);
      {
        const bool isv = (u >= 4) && (it == 7);
        const int nt = isv ? (u - 4) : (it * 8 + u + 4);
        const rsrc_t rr = mk(isv ? (const void*)(p.vc + kvbase) : (const void*)(p.kc + kvbase), KV_WG_BYTES);
#pragma unroll
        for (int i = 0; i < 3; ++i) ring[slot][i] = ld4<AUX_NT>(rr, voff + i * 128, tile0 + nt * 24576);
      }
      __builtin_amdgcn_sched_barrier(0);
      float d = (d0 + d1) + d2;
      d += dpp<XOR1>(d); d += dpp<XOR2>(d); d += dpp<HMIR>(d);
      const float s = rbf(rbf(d) * 0.07216878364870322f) * 1.4426950408889634f;
      sreg = (c == u) ? s : sreg;
    }
    mx = fmaxf(mx, sreg);
    sc[it * 64 + lane] = sreg;
  }
  STAMP(7);
  const float mw = wave_max(mx);
  {
    float l = 0.f;
#pragma unroll
    for (int it = 0; it < 8; ++it) {
      const float pe = __builtin_amdgcn_exp2f(sc[it * 64 + lane] - mw);
      l += pe;
      sc[it * 64 + lane] = pe;
    }
    l = wave_sum(l);
    if (lane == 0) {
      const u32x4 gr = {__float_as_uint(mw), tag, __float_as_uint(l), tag};
      __builtin_amdgcn_raw_buffer_store_b128(gr, rws, WS_ML + ((b * NH + h) * 16 + half * 8 + wave) * 16, 0, AUX_SC1);
    }
  }
  const int g4 = lane >> 4, n16 = lane & 15;
  if (ffn_wg && wave < 5) {
    u32x4 bf[6];
    const int ho = WS_HB + ((xcd * 4 + (lane & 3)) * 96 + 4 * g4) * 8;
    for (unsigned spins = 0; spins < SPIN_CAP; ++spins) {
      asm volatile("" ::: "memory");
      bool ok = true;
#pragma unroll
      for (int s = 0; s < 6; ++s) {
        const u32x4 a0 = ld4<AUX_SC1>(rws, ho + s * 128, 0), a1 = ld4<AUX_SC1>(rws, ho + s * 128 + 16, 0);
        ok = ok && a0[1] == tag && a0[3] == tag && a1[1] == tag && a1[3] == tag;
        bf[s] = u32x4{a0[0], a0[2], a1[0], a1[2]};
      }
      if (__all(ok)) break;
      __builtin_amdgcn_s_sleep(2);
    }
    f32x4 acc = {0.f, 0.f, 0.f, 0.f};
    const LAS unsigned short* wl = (const LAS unsigned short*)(L + L_W2) + wave * 16 + n16;
#pragma unroll
    for (int s = 0; s < 6; ++s) {
      u32x4 af;
#pragma unroll
      for (int j = 0; j < 4; ++j) {
        const int k = 32 * s + 8 * g4 + 2 * j;
        af[j] = (unsigned)wl[k * 80] | ((unsigned)wl[(k + 1) * 80] << 16);
      }
      acc = __builtin_amdgcn_mfma_f32_16x16x32_bf16(__builtin_bit_cast(bf16x8, af), __builtin_bit_cast(bf16x8, bf[s]), acc, 0, 0, 0);
    }
    if (n16 < 4) {
      const int n = 16 * wave + 4 * g4, q = n / 20, ix = n % 20;
      const int o = WS_YF + ((((bs * 4 + n16) * 4 + q) * 8 + xcd) * 20 + ix) * 8;
      __builtin_amdgcn_raw_buffer_store_b128(u32x4{__float_as_uint(acc[0]), tag, __float_as_uint(acc[1]), tag}, rws, o, 0, AUX_SC1);
      __builtin_amdgcn_raw_buffer_store_b128(u32x4{__float_as_uint(acc[2]), tag, __float_as_uint(acc[3]), tag}, rws, o + 16, 0, AUX_SC1);
    }
  }
  STAMP(8);

  float acc[24];
#pragma unroll
  for (int i = 0; i < 24; ++i) acc[i] = 0.f;
  u32x4 ml = {0u, 0u, 0u, 0u};
  for (int it = 0; it < 8; ++it) {
    const f32x4 pa = *(const LAS f32x4*)(sc + it * 64 + r * 8), pb = *(const LAS f32x4*)(sc + it * 64 + r * 8 + 4);
#pragma unroll
    for (int u = 0; u < 8; ++u) {
      const int slot = u & 3;
      const float pe = u < 4 ? pa[u] : pb[u - 4];
#pragma unroll
      for (int i = 0; i < 3; ++i)
#pragma unroll
        for (int j = 0; j < 4; ++j) {
          acc[i * 8 + 2 * j] = fmaf(pe, bflo(ring[slot][i][j]), acc[i * 8 + 2 * j]);
          acc[i * 8 + 2 * j + 1] = fmaf(pe, bfhi(ring[slot][i][j]), acc[i * 8 + 2 * j + 1]);
        }
      __builtin_amdgcn_sched_barrier(0);
      {
        const bool dead = (u >= 4) && (it == 7);
        const rsrc_t rr = mk(p.vc + kvbase, dead ? 0u : KV_WG_BYTES);
#pragma unroll
        for (int i = 0; i < 3; ++i) ring[slot][i] = ld4<AUX_NT>(rr, voff + i * 128, tile0 + (it * 8 + u + 4) * 24576);
      }
      __builtin_amdgcn_sched_barrier(0);
    }
  }
  STAMP(9);
  {
    const int mo = WS_ML + ((b * NH + h) * 16 + n16) * 16;
    for (unsigned spins = 0; spins < SPIN_CAP; ++spins) {
      asm volatile("" ::: "memory");
      ml = ld4<AUX_SC1>(rws, mo, 0);
      if (__all(ml[1] == tag && ml[3] == tag)) break;
      __builtin_amdgcn_s_sleep(2);
    }
    const float mi = __uint_as_float(ml[0]), li = __uint_as_float(ml[2]);
    const float M = row16_max(mi);
    const float Lsum = row16_sum(li * __builtin_amdgcn_exp2f(mi - M));
    const float f = __builtin_amdgcn_exp2f(mw - M) / Lsum;
#pragma unroll
    for (int i = 0; i < 24; ++i) acc[i] *= f;
    float o[3];
    reduce24(acc, o, lane);
    const int base = 12 * (lane >> 5) + 6 * ((lane >> 4) & 1) + 3 * ((lane >> 3) & 1);
    LAS float* red = (LAS float*)(L + L_RED) + wave * 192;
#pragma unroll
    for (int i = 0; i < 3; ++i) { const int ai = base + i; red[(c + 8 * (ai >> 3)) * 8 + (ai & 7)] = o[i]; }
  }
  __syncthreads();
  if (tid < 192) {
    const LAS float* red = (const LAS float*)(L + L_RED);
    float s = 0.f;
#pragma unroll
    for (int v = 0; v < 8; ++v) s += red[v * 192 + tid];
    __builtin_amdgcn_raw_buffer_store_b64(u32x2{__float_as_uint(s), tag}, rws, WS_O + (((h * 4 + b) * 192 + tid) * 2 + half) * 8, 0, AUX_SC1);
  }
  STAMP(10);

  if (wave < 6) {
    u32x4 af[6];
    if (wave < 5) {
      const LAS unsigned short* wl = (const LAS unsigned short*)(L + L_WO) + wave * 16 + n16;
#pragma unroll
      for (int s = 0; s < 6; ++s)
#pragma unroll
        for (int j = 0; j < 4; ++j) {
          const int k = 32 * s + 8 * g4 + 2 * j;
          af[s][j] = (unsigned)wl[k * 80] | ((unsigned)wl[(k + 1) * 80] << 16);
        }
    }
    const int bb = tid / 96, dp = tid % 96;
    const int oo = WS_O + ((h * 4 + bb) * 192 + 2 * dp) * 16;
    u32x4 a0, a1;
    for (unsigned spins = 0; spins < SPIN_CAP; ++spins) {
      asm volatile("" ::: "memory");
      a0 = ld4<AUX_SC1>(rws, oo, 0); a1 = ld4<AUX_SC1>(rws, oo + 16, 0);
      if (__all(a0[1] == tag && a0[3] == tag && a1[1] == tag && a1[3] == tag)) break;
      __builtin_amdgcn_s_sleep(1);
    }
    ((LAS unsigned*)(L + L_OB))[tid] =
        cvtpk(__uint_as_float(a0[0]) + __uint_as_float(a0[2]), __uint_as_float(a1[0]) + __uint_as_float(a1[2]));
    LAS unsigned* cnt = (LAS unsigned*)(L + L_MISC) + 1;
    asm volatile("s_waitcnt lgkmcnt(0)" ::: "memory");
    if (lane == 0) __hip_atomic_fetch_add(cnt, 1u, __ATOMIC_RELAXED, __HIP_MEMORY_SCOPE_WORKGROUP);
    if (wave < 5) {
      for (unsigned spins = 0; spins < SPIN_CAP; ++spins) {
        if (__hip_atomic_load(cnt, __ATOMIC_RELAXED, __HIP_MEMORY_SCOPE_WORKGROUP) >= 6u) break;
      }
      asm volatile("" ::: "memory");
      f32x4 y = {0.f, 0.f, 0.f, 0.f};
#pragma unroll
      for (int s = 0; s < 6; ++s) {
        const u32x4 bfr = *(const LAS u32x4*)(L + L_OB + (lane & 3) * 384 + (32 * s + 8 * g4) * 2);
        y = __builtin_amdgcn_mfma_f32_16x16x32_bf16(__builtin_bit_cast(bf16x8, af[s]), __builtin_bit_cast(bf16x8, bfr), y, 0, 0, 0);
      }
      if (n16 < 4) {
        const int n = 16 * wave + 4 * g4, q = n / 20, ix = n % 20;
        const int o = WS_Y + ((((bs * 4 + n16) * 4 + q) * 32 + h) * 20 + ix) * 8;
        __builtin_amdgcn_raw_buffer_store_b128(u32x4{__float_as_uint(y[0]), tag, __float_as_uint(y[1]), tag}, rws, o, 0, AUX_SC1);
        __builtin_amdgcn_raw_buffer_store_b128(u32x4{__float_as_uint(y[2]), tag, __float_as_uint(y[3]), tag}, rws, o + 16, 0, AUX_SC1);
      }
    }
    STAMP(11);
  } else if (wave == 7 && h < 16) {
    const int fb = h & 3, fq = h >> 2;
    float gs = 0.f;
    {
      const rsrc_t rg = mk(p.gw, DM * 2);
#pragma unroll
      for (int i = 0; i < 5; ++i) {
        const unsigned xv = __builtin_amdgcn_raw_buffer_load_b32(rx, (fb * DM + 2 * lane + 128 * i) * 2, 0, 0);
        const unsigned gv = __builtin_amdgcn_raw_buffer_load_b32(rg, (2 * lane + 128 * i) * 2, 0, 0);
        gs += rbf(bflo(xv) * bflo(gv)) + rbf(bfhi(xv) * bfhi(gv));
      }
      gs = wave_sum(gs);
    }
    const float gate = rbf(1.f / (1.f + __expf(-rbf(gs))));
    const int hh = lane & 31, e2 = lane >> 5;
    float fs[10], as[10];
    {
      const int o = hh < 8 ? WS_YF + ((((bs * 4 + fb) * 4 + fq) * 8 + hh) * 20 + 10 * e2) * 8 : oob;
      for (unsigned spins = 0; spins < SPIN_CAP; ++spins) {
        asm volatile("" ::: "memory");
        bool ok = true;
#pragma unroll
        for (int i = 0; i < 5; ++i) {
          const u32x4 v = ld4<AUX_SC1>(rws, o + 16 * i, 0);
          ok = ok && v[1] == tag && v[3] == tag;
          fs[2 * i] = __uint_as_float(v[0]); fs[2 * i + 1] = __uint_as_float(v[2]);
        }
        if (__all(ok || hh >= 8)) break;
        __builtin_amdgcn_s_sleep(1);
      }
#pragma unroll
      for (int i = 0; i < 10; ++i) { float t = row16_sum(fs[i]); fs[i] = swap16_add(t, t); }
    }
    {
      const int o = WS_Y + ((((bs * 4 + fb) * 4 + fq) * 32 + hh) * 20 + 10 * e2) * 8;
      for (unsigned spins = 0; spins < SPIN_CAP; ++spins) {
        asm volatile("" ::: "memory");
        bool ok = true;
#pragma unroll
        for (int i = 0; i < 5; ++i) {
          const u32x4 v = ld4<AUX_SC1>(rws, o + 16 * i, 0);
          ok = ok && v[1] == tag && v[3] == tag;
          as[2 * i] = __uint_as_float(v[0]); as[2 * i + 1] = __uint_as_float(v[2]);
        }
        if (__all(ok)) break;
        __builtin_amdgcn_s_sleep(1);
      }
#pragma unroll
      for (int i = 0; i < 10; ++i) { float t = row16_sum(as[i]); as[i] = swap16_add(t, t); }
    }
    float av = as[0], fv = fs[0];
#pragma unroll
    for (int i = 1; i < 10; ++i) { av = hh == i ? as[i] : av; fv = hh == i ? fs[i] : fv; }
    const float res = rbf(gate * rbf(av)) + rbf(rbf(1.f - gate) * rbf(fv));
    if (hh < 10) p.out[fb * DM + 80 * bs + 20 * fq + 10 * e2 + hh] = (unsigned short)(cvtpk(res, res) & 0xffffu);
    if (w == 0 && lane == 0) __builtin_amdgcn_raw_buffer_store_b32(tag, rws, WS_E, 0, AUX_SC1);
    STAMP(12);
  }
  STAMP_FLUSH;
}

}

extern "C" void kernel_launch(void* const* d_in, const int* in_sizes, int n_in, void* d_out, int out_size, void* d_ws,
                              size_t ws_size, hipStream_t stream) {
  Params p;
  p.x = (const unsigned short*)d_in[0];
  p.kc = (const unsigned short*)d_in[1];
  p.vc = (const unsigned short*)d_in[2];
  p.wq = (const unsigned short*)d_in[3];
  p.wo = (const unsigned short*)d_in[4];
  p.w1 = (const unsigned short*)d_in[6];
  p.w3 = (const unsigned short*)d_in[7];
  p.w2 = (const unsigned short*)d_in[8];
  p.gw = (const unsigned short*)d_in[9];
  p.out = (unsigned short*)d_out;
  p.ws = (unsigned char*)d_ws;
  (void)hipFuncSetAttribute((const void*)fused_decode, hipFuncAttributeMaxDynamicSharedMemorySize, L_END);
  fused_decode<<<256, NTHR, L_END, stream>>>(p);
}
```

```cpp
#include <hip/hip_runtime.h>
#include <stdint.h>

namespace {

typedef unsigned u32x4 __attribute__((ext_vector_type(4)));
typedef unsigned u32x3 __attribute__((ext_vector_type(3)));
typedef unsigned u32x2 __attribute__((ext_vector_type(2)));
typedef float f32x4 __attribute__((ext_vector_type(4)));
typedef __bf16 bf16x2 __attribute__((ext_vector_type(2)));
typedef __bf16 bf16x8 __attribute__((ext_vector_type(8)));
#define LAS __attribute__((address_space(3)))

constexpr int DM = 640, NH = 32, SK = 8192, DH = 192, FF = 1536;
constexpr int NTHR = 512;
constexpr unsigned KV_WG_BYTES = 4096u * 384u;
constexpr int AUX_NT = 2, AUX_SC1 = 16;
constexpr int RSRC_W3 = 0x00020000;

constexpr int WS_E = 0;
constexpr int WS_ML = 4096;
constexpr int WS_HB = WS_ML + 128 * 16 * 16;
constexpr int WS_O = WS_HB + 8 * 4 * 96 * 8;
constexpr int WS_Y = WS_O + 32 * 4 * 192 * 16;
constexpr int WS_YF = WS_Y + 8 * 4 * 4 * 32 * 160;
constexpr int WS_END = WS_YF + 8 * 4 * 4 * 8 * 160;

constexpr int L_SC = 0;
constexpr int L_RED = L_SC + 8 * 512 * 4;
constexpr int L_FRED = L_RED + 8 * 192 * 4;
constexpr int L_QB = L_FRED + 8 * 48 * 4;
constexpr int L_OB = L_QB + 384;
constexpr int L_MISC = L_OB + 4 * 384;
constexpr int L_WO = L_MISC + 64;
constexpr int L_W2 = L_WO + 192 * 160;
constexpr int L_END = L_W2 + 192 * 160;
static_assert(L_END > 80 * 1024 && L_END <= 160 * 1024, "one workgroup per CU");

constexpr unsigned SPIN_CAP = 1u << 22;

__device__ __forceinline__ float bflo(unsigned u) { return __uint_as_float(u << 16); }
__device__ __forceinline__ float bfhi(unsigned u) { return __uint_as_float(u & 0xffff0000u); }
__device__ __forceinline__ unsigned cvtpk(float lo, float hi) {
  bf16x2 r = {(__bf16)lo, (__bf16)hi};
  return __builtin_bit_cast(unsigned, r);
}
__device__ __forceinline__ float rbf(float f) { return bflo(cvtpk(f, f) & 0xffffu); }
__device__ __forceinline__ float dot2(unsigned a, unsigned b, float c) {
  return __builtin_amdgcn_fdot2_f32_bf16(__builtin_bit_cast(bf16x2, a), __builtin_bit_cast(bf16x2, b), c, false);
}
template <int CTRL> __device__ __forceinline__ float dpp(float x) {
  return __builtin_bit_cast(float, __builtin_amdgcn_mov_dpp(__builtin_bit_cast(int, x), CTRL, 0xf, 0xf, true));
}
constexpr int XOR1 = 0xB1, XOR2 = 0x4E, HMIR = 0x141, MIR = 0x140, ROR8 = 0x128;
__device__ __forceinline__ float swap32_add(float a, float b) {
  auto s = __builtin_amdgcn_permlane32_swap(__float_as_uint(a), __float_as_uint(b), false, false);
  return __uint_as_float(s[0]) + __uint_as_float(s[1]);
}
__device__ __forceinline__ float swap16_add(float a, float b) {
  auto s = __builtin_amdgcn_permlane16_swap(__float_as_uint(a), __float_as_uint(b), false, false);
  return __uint_as_float(s[0]) + __uint_as_float(s[1]);
}
__device__ __forceinline__ float swap32_max(float a) {
  auto s = __builtin_amdgcn_permlane32_swap(__float_as_uint(a), __float_as_uint(a), false, false);
  return fmaxf(__uint_as_float(s[0]), __uint_as_float(s[1]));
}
__device__ __forceinline__ float swap16_max(float a) {
  auto s = __builtin_amdgcn_permlane16_swap(__float_as_uint(a), __float_as_uint(a), false, false);
  return fmaxf(__uint_as_float(s[0]), __uint_as_float(s[1]));
}
__device__ __forceinline__ float row16_sum(float x) { x += dpp<XOR1>(x); x += dpp<XOR2>(x); x += dpp<HMIR>(x); x += dpp<MIR>(x); return x; }
__device__ __forceinline__ float row16_max(float x) {
  x = fmaxf(x, dpp<XOR1>(x)); x = fmaxf(x, dpp<XOR2>(x)); x = fmaxf(x, dpp<HMIR>(x)); x = fmaxf(x, dpp<MIR>(x)); return x;
}
__device__ __forceinline__ float wave_sum(float x) { x = row16_sum(x); x = swap16_add(x, x); return swap32_add(x, x); }
__device__ __forceinline__ float wave_max(float x) { x = row16_max(x); x = swap16_max(x); return swap32_max(x); }

typedef __amdgpu_buffer_rsrc_t rsrc_t;
__device__ __forceinline__ rsrc_t mk(const void* p, unsigned bytes) { return __builtin_amdgcn_make_buffer_rsrc((void*)p, 0, bytes, RSRC_W3); }
template <int AUX> __device__ __forceinline__ u32x4 ld4(rsrc_t r, int voff, int soff) { return __builtin_amdgcn_raw_buffer_load_b128(r, voff, soff, AUX); }

__device__ __forceinline__ void reduce24(const float (&a)[24], float (&o)[3], int lane) {
  float t[12], u[6];
#pragma unroll
  for (int i = 0; i < 12; ++i) t[i] = swap32_add(a[i], a[i + 12]);
#pragma unroll
  for (int i = 0; i < 6; ++i) u[i] = swap16_add(t[i], t[i + 6]);
#pragma unroll
  for (int i = 0; i < 3; ++i) {
    const float x = u[i] + dpp<ROR8>(u[i]), y = u[i + 3] + dpp<ROR8>(u[i + 3]);
    o[i] = (lane & 8) ? y : x;
  }
}

__device__ __forceinline__ void stage_slice(const unsigned short* src, int dst, int wave, int lane) {
#pragma unroll
  for (int u = 0; u < 4; ++u) {
    const int blk = u * 8 + wave;
    if (blk < 30) {
      const int id = blk * 64 + lane;
      const unsigned short* g = src + (id / 10) * DM + (id % 10) * 8;
      unsigned keep;
      asm volatile("s_mov_b32 %0, m0\n\ts_mov_b32 m0, %2\n\ts_nop 0\n\tglobal_load_lds_dwordx4 %1, off\n\ts_mov_b32 m0, %0"
                   : "=&s"(keep) : "v"(g), "s"(dst + blk * 1024) : "memory");
    }
  }
}

struct Params {
  const unsigned short* x;
  const unsigned short* kc;
  const unsigned short* vc;
  const unsigned short* wq;
  const unsigned short* wo;
  const unsigned short* w1;
  const unsigned short* w3;
  const unsigned short* w2;
  const unsigned short* gw;
  unsigned short* out;
  unsigned char* ws;
};

#define STAMP(i) do { } while (0)
#define STAMP_DECL do { } while (0)
#define STAMP_FLUSH do { } while (0)

__global__ void __launch_bounds__(NTHR, 2) fused_decode(Params p) {
  extern __shared__ __attribute__((aligned(16))) unsigned char lds_raw[];
  LAS unsigned char* L = (LAS unsigned char*)lds_raw;
  const int tid = threadIdx.x, lane = tid & 63;
  const int wave = __builtin_amdgcn_readfirstlane(tid >> 6);
  const int r = lane >> 3, c = lane & 7;
  const int w = blockIdx.x, xcd = w & 7, i32 = w >> 3;
  const int h = xcd * 4 + (i32 & 3), bs = i32 >> 2, b = bs >> 1, half = bs & 1;
  const bool ffn_wg = (i32 & 3) == 0;
  const unsigned long long tstart_ = __builtin_amdgcn_s_memrealtime();
  STAMP_DECL;
  STAMP(0);

  const rsrc_t rws = mk(p.ws, WS_END);
  const size_t kvbase = ((size_t)(b * NH + h) * SK + (size_t)half * 4096) * DH;
  const rsrc_t rk = mk(p.kc + kvbase, KV_WG_BYTES);
  const rsrc_t rv = mk(p.vc + kvbase, KV_WG_BYTES);
  const int voff = r * 384 + c * 16;
  const int tile0 = wave * 3072;

  u32x4 ring[4][3];
#pragma unroll
  for (int u = 0; u < 4; ++u)
#pragma unroll
    for (int i = 0; i < 3; ++i) ring[u][i] = ld4<AUX_NT>(rk, voff + i * 128, tile0 + u * 24576);
  __builtin_amdgcn_sched_barrier(0);

  const unsigned epoch_in = __builtin_amdgcn_raw_buffer_load_b32(rws, WS_E, 0, AUX_SC1);
  if (ffn_wg) stage_slice(p.w2 + (size_t)xcd * DH * DM + bs * 80, L_W2, wave, lane);
  const rsrc_t rx = mk(p.x, 4 * DM * 2);
  const int xo = (b * DM + 80 * wave + 10 * r) * 2;
  const u32x4 xq4 = ld4<0>(rx, xo, 0);
  const unsigned xq1 = __builtin_amdgcn_raw_buffer_load_b32(rx, xo + 16, 0, 0);
  const int f0 = xcd * 192 + i32 * 6;
  const int k1 = 80 * wave + lane;
  const int oob = 0x40000000;
  const int fo1 = (k1 * FF + f0) * 2, fo2 = lane < 16 ? fo1 + 64 * FF * 2 : oob;
  const rsrc_t r1 = mk(p.w1, DM * FF * 2), r3 = mk(p.w3, DM * FF * 2);
  u32x3 wa[2], wb[2];
  wa[0] = __builtin_amdgcn_raw_buffer_load_b96(r1, fo1, 0, 0);
  wb[0] = __builtin_amdgcn_raw_buffer_load_b96(r3, fo1, 0, 0);
  wa[1] = __builtin_amdgcn_raw_buffer_load_b96(r1, fo2, 0, 0);
  wb[1] = __builtin_amdgcn_raw_buffer_load_b96(r3, fo2, 0, 0);
  unsigned xf[2][4];
#pragma unroll
  for (int bb = 0; bb < 4; ++bb) {
    xf[0][bb] = __builtin_amdgcn_raw_buffer_load_b16(rx, (bb * DM + k1) * 2, 0, 0);
    xf[1][bb] = __builtin_amdgcn_raw_buffer_load_b16(rx, lane < 16 ? (bb * DM + k1 + 64) * 2 : oob, 0, 0);
  }
  const rsrc_t rq = mk(p.wq + h * DH, (DM * NH * DH - h * DH) * 2);
  const int qo = r * (10 * NH * DH * 2) + c * 16;
  u32x4 wqv[10][3];
#pragma unroll
  for (int t = 0; t < 10; ++t)
#pragma unroll
    for (int i = 0; i < 3; ++i) wqv[t][i] = ld4<0>(rq, qo + i * 128, (80 * wave + t) * (NH * DH * 2));
  __builtin_amdgcn_sched_barrier(0);

  STAMP(1);
  {
    float a[24];
#pragma unroll
    for (int i = 0; i < 24; ++i) a[i] = 0.f;
#pragma unroll
    for (int t = 0; t < 10; ++t) {
      const unsigned xw = t < 8 ? xq4[t >> 1] : xq1;
      const unsigned xl = (t & 1) ? (xw >> 16) : (xw & 0xffffu), xh = (t & 1) ? (xw & 0xffff0000u) : (xw << 16);
#pragma unroll
      for (int i = 0; i < 3; ++i)
#pragma unroll
        for (int j = 0; j < 4; ++j) {
          a[i * 8 + 2 * j] = dot2(wqv[t][i][j], xl, a[i * 8 + 2 * j]);
          a[i * 8 + 2 * j + 1] = dot2(wqv[t][i][j], xh, a[i * 8 + 2 * j + 1]);
        }
    }
    float o[3];
    reduce24(a, o, lane);
    const int base = 12 * (lane >> 5) + 6 * ((lane >> 4) & 1) + 3 * ((lane >> 3) & 1);
    LAS float* red = (LAS float*)(L + L_RED) + wave * 192;
#pragma unroll
    for (int i = 0; i < 3; ++i) { const int ai = base + i; red[(c + 8 * (ai >> 3)) * 8 + (ai & 7)] = o[i]; }
  }
  __builtin_amdgcn_sched_barrier(0);
  STAMP(2);
  stage_slice(p.wo + (size_t)h * DH * DM + bs * 80, L_WO, wave, lane);
  __builtin_amdgcn_sched_barrier(0);
  {
    float g[24], u_[24];
#pragma unroll
    for (int i = 0; i < 24; ++i) { g[i] = 0.f; u_[i] = 0.f; }
#pragma unroll
    for (int rr = 0; rr < 2; ++rr)
#pragma unroll
      for (int bb = 0; bb < 4; ++bb) {
        const unsigned xl = xf[rr][bb] & 0xffffu, xh = xl << 16;
#pragma unroll
        for (int j = 0; j < 3; ++j) {
          g[bb * 6 + 2 * j] = dot2(wa[rr][j], xl, g[bb * 6 + 2 * j]);
          g[bb * 6 + 2 * j + 1] = dot2(wa[rr][j], xh, g[bb * 6 + 2 * j + 1]);
          u_[bb * 6 + 2 * j] = dot2(wb[rr][j], xl, u_[bb * 6 + 2 * j]);
          u_[bb * 6 + 2 * j + 1] = dot2(wb[rr][j], xh, u_[bb * 6 + 2 * j + 1]);
        }
      }
    float t[24], s12[12], s6[6], s3[3];
#pragma unroll
    for (int i = 0; i < 24; ++i) t[i] = swap32_add(g[i], u_[i]);
#pragma unroll
    for (int i = 0; i < 12; ++i) s12[i] = swap16_add(t[i], t[i + 12]);
#pragma unroll
    for (int i = 0; i < 6; ++i) {
      const float x = s12[i] + dpp<ROR8>(s12[i]), y = s12[i + 6] + dpp<ROR8>(s12[i + 6]);
      s6[i] = (lane & 8) ? y : x;
    }
#pragma unroll
    for (int i = 0; i < 3; ++i) {
      float x = s6[i], y = s6[i + 3];
      x += dpp<XOR1>(x); x += dpp<XOR2>(x); x += dpp<HMIR>(x);
      y += dpp<XOR1>(y); y += dpp<XOR2>(y); y += dpp<HMIR>(y);
      s3[i] = (lane & 4) ? y : x;
    }
    if ((lane & 3) == 0) {
      const int idx = 24 * (lane >> 5) + 12 * ((lane >> 4) & 1) + 6 * ((lane >> 3) & 1) + 3 * ((lane >> 2) & 1);
      LAS float* fr = (LAS float*)(L + L_FRED) + wave * 48 + idx;
      fr[0] = s3[0]; fr[1] = s3[1]; fr[2] = s3[2];
    }
  }
  STAMP(3);
  if (tid == 0) { ((LAS unsigned*)(L + L_MISC))[0] = epoch_in + 1u; ((LAS unsigned*)(L + L_MISC))[1] = 0u; }
  __syncthreads();
  STAMP(4);
  const unsigned tag = __builtin_amdgcn_readfirstlane(((LAS unsigned*)(L + L_MISC))[0]);
  if (tid < 192) {
    const LAS float* red = (const LAS float*)(L + L_RED);
    float s = 0.f;
#pragma unroll
    for (int v = 0; v < 8; ++v) s += red[v * 192 + tid];
    ((LAS unsigned short*)(L + L_QB))[tid] = (unsigned short)(cvtpk(s, s) & 0xffffu);
  } else if (tid >= 256 && tid < 256 + 12) {
    const int i = tid - 256, bb = i / 3, jp = i % 3;
    const LAS float* fr = (const LAS float*)(L + L_FRED);
    float hv[2];
#pragma unroll
    for (int e = 0; e < 2; ++e) {
      float g = 0.f, u = 0.f;
#pragma unroll
      for (int v = 0; v < 8; ++v) { g += fr[v * 48 + bb * 6 + 2 * jp + e]; u += fr[v * 48 + 24 + bb * 6 + 2 * jp + e]; }
      g = rbf(g); u = rbf(u);
      const float s = rbf(g / (1.f + __expf(-g)));
      hv[e] = s * u;
    }
    const u32x2 gr = {cvtpk(hv[0], hv[1]), tag};
    __builtin_amdgcn_raw_buffer_store_b64(gr, rws, WS_HB + ((xcd * 4 + bb) * 96 + i32 * 3 + jp) * 8, 0, AUX_SC1);
  }
  STAMP(5);
  __syncthreads();
  u32x4 qv[3];
#pragma unroll
  for (int i = 0; i < 3; ++i) qv[i] = *(const LAS u32x4*)(L + L_QB + (c + 8 * i) * 16);
  STAMP(6);

  LAS float* sc = (LAS float*)(L + L_SC) + wave * 512;
  float mx = -INFINITY;
  for (int it = 0; it < 8; ++it) {
    float sreg = 0.f;
#pragma unroll
    for (int u = 0; u < 8; ++u) {
      const int slot = u & 3;
      float d0 = 0.f, d1 = 0.f, d2 = 0.f;
#pragma unroll
      for (int j = 0; j < 4; ++j) {
        d0 = dot2(ring[slot][0][j], qv[0][j], d0);
        d1 = dot2(ring[slot][1][j], qv[1][j], d1);
        d2 = dot2(ring[slot][2][j], qv[2][j], d2);
      }
      __builtin_amdgcn_sched_barrier(0);
      {
        const bool isv = (u >= 4) && (it == 7);
        const int nt = isv ? (u - 4) : (it * 8 + u + 4);
        const rsrc_t rr = mk(isv ? (const void*)(p.vc + kvbase) : (const void*)(p.kc + kvbase), KV_WG_BYTES);
#pragma unroll
        for (int i = 0; i < 3; ++i) ring[slot][i] = ld4<AUX_NT>(rr, voff + i * 128, tile0 + nt * 24576);
      }
      __builtin_amdgcn_sched_barrier(0);
      float d = (d0 + d1) + d2;
      d += dpp<XOR1>(d); d += dpp<XOR2>(d); d += dpp<HMIR>(d);
      const float s = rbf(rbf(d) * 0.07216878364870322f) * 1.4426950408889634f;
      sreg = (c == u) ? s : sreg;
    }
    mx = fmaxf(mx, sreg);
    sc[it * 64 + lane] = sreg;
  }
  STAMP(7);
  const float mw = wave_max(mx);
  {
    float l = 0.f;
#pragma unroll
    for (int it = 0; it < 8; ++it) {
      const float pe = __builtin_amdgcn_exp2f(sc[it * 64 + lane] - mw);
      l += pe;
      sc[it * 64 + lane] = pe;
    }
    l = wave_sum(l);
    if (lane == 0) {
      const u32x4 gr = {__float_as_uint(mw), tag, __float_as_uint(l), tag};
      __builtin_amdgcn_raw_buffer_store_b128(gr, rws, WS_ML + ((b * NH + h) * 16 + half * 8 + wave) * 16, 0, AUX_SC1);
    }
  }
  const int g4 = lane >> 4, n16 = lane & 15;
  if (ffn_wg && wave < 5) {
    u32x4 bf[6];
    const int ho = WS_HB + ((xcd * 4 + (lane & 3)) * 96 + 4 * g4) * 8;
    for (unsigned spins = 0; spins < SPIN_CAP; ++spins) {
      asm volatile("" ::: "memory");
      bool ok = true;
#pragma unroll
      for (int s = 0; s < 6; ++s) {
        const u32x4 a0 = ld4<AUX_SC1>(rws, ho + s * 128, 0), a1 = ld4<AUX_SC1>(rws, ho + s * 128 + 16, 0);
        ok = ok && a0[1] == tag && a0[3] == tag && a1[1] == tag && a1[3] == tag;
        bf[s] = u32x4{a0[0], a0[2], a1[0], a1[2]};
      }
      if (__all(ok)) break;
      __builtin_amdgcn_s_sleep(2);
    }
    f32x4 acc = {0.f, 0.f, 0.f, 0.f};
    const LAS unsigned short* wl = (const LAS unsigned short*)(L + L_W2) + wave * 16 + n16;
#pragma unroll
    for (int s = 0; s < 6; ++s) {
      u32x4 af;
#pragma unroll
      for (int j = 0; j < 4; ++j) {
        const int k = 32 * s + 8 * g4 + 2 * j;
        af[j] = (unsigned)wl[k * 80] | ((unsigned)wl[(k + 1) * 80] << 16);
      }
      acc = __builtin_amdgcn_mfma_f32_16x16x32_bf16(__builtin_bit_cast(bf16x8, af), __builtin_bit_cast(bf16x8, bf[s]), acc, 0, 0, 0);
    }
    if (n16 < 4) {
      const int n = 16 * wave + 4 * g4, q = n / 20, ix = n % 20;
      const int o = WS_YF + ((((bs * 4 + n16) * 4 + q) * 8 + xcd) * 20 + ix) * 8;
      __builtin_amdgcn_raw_buffer_store_b128(u32x4{__float_as_uint(acc[0]), tag, __float_as_uint(acc[1]), tag}, rws, o, 0, AUX_SC1);
      __builtin_amdgcn_raw_buffer_store_b128(u32x4{__float_as_uint(acc[2]), tag, __float_as_uint(acc[3]), tag}, rws, o + 16, 0, AUX_SC1);
    }
  }
  STAMP(8);

  float acc[24];
#pragma unroll
  for (int i = 0; i < 24; ++i) acc[i] = 0.f;
  u32x4 ml = {0u, 0u, 0u, 0u};
  for (int it = 0; it < 8; ++it) {
    const f32x4 pa = *(const LAS f32x4*)(sc + it * 64 + r * 8), pb = *(const LAS f32x4*)(sc + it * 64 + r * 8 + 4);
#pragma unroll
    for (int u = 0; u < 8; ++u) {
      const int slot = u & 3;
      const float pe = u < 4 ? pa[u] : pb[u - 4];
#pragma unroll
      for (int i = 0; i < 3; ++i)
#pragma unroll
        for (int j = 0; j < 4; ++j) {
          acc[i * 8 + 2 * j] = fmaf(pe, bflo(ring[slot][i][j]), acc[i * 8 + 2 * j]);
          acc[i * 8 + 2 * j + 1] = fmaf(pe, bfhi(ring[slot][i][j]), acc[i * 8 + 2 * j + 1]);
        }
      __builtin_amdgcn_sched_barrier(0);
      {
        const bool dead = (u >= 4) && (it == 7);
        const rsrc_t rr = mk(p.vc + kvbase, dead ? 0u : KV_WG_BYTES);
#pragma unroll
        for (int i = 0; i < 3; ++i) ring[slot][i] = ld4<AUX_NT>(rr, voff + i * 128, tile0 + (it * 8 + u + 4) * 24576);
      }
      __builtin_amdgcn_sched_barrier(0);
    }
  }
  STAMP(9);
  {
    const int mo = WS_ML + ((b * NH + h) * 16 + n16) * 16;
    for (unsigned spins = 0; spins < SPIN_CAP; ++spins) {
      asm volatile("" ::: "memory");
      ml = ld4<AUX_SC1>(rws, mo, 0);
      if (__all(ml[1] == tag && ml[3] == tag)) break;
      __builtin_amdgcn_s_sleep(2);
    }
    const float mi = __uint_as_float(ml[0]), li = __uint_as_float(ml[2]);
    const float M = row16_max(mi);
    const float Lsum = row16_sum(li * __builtin_amdgcn_exp2f(mi - M));
    const float f = __builtin_amdgcn_exp2f(mw - M) / Lsum;
#pragma unroll
    for (int i = 0; i < 24; ++i) acc[i] *= f;
    float o[3];
    reduce24(acc, o, lane);
    const int base = 12 * (lane >> 5) + 6 * ((lane >> 4) & 1) + 3 * ((lane >> 3) & 1);
    LAS float* red = (LAS float*)(L + L_RED) + wave * 192;
#pragma unroll
    for (int i = 0; i < 3; ++i) { const int ai = base + i; red[(c + 8 * (ai >> 3)) * 8 + (ai & 7)] = o[i]; }
  }
  __syncthreads();
  if (tid < 192) {
    const LAS float* red = (const LAS float*)(L + L_RED);
    float s = 0.f;
#pragma unroll
    for (int v = 0; v < 8; ++v) s += red[v * 192 + tid];
    __builtin_amdgcn_raw_buffer_store_b64(u32x2{__float_as_uint(s), tag}, rws, WS_O + (((h * 4 + b) * 192 + tid) * 2 + half) * 8, 0, AUX_SC1);
  }
  STAMP(10);

  if (wave < 6) {
    u32x4 af[6];
    if (wave < 5) {
      const LAS unsigned short* wl = (const LAS unsigned short*)(L + L_WO) + wave * 16 + n16;
#pragma unroll
      for (int s = 0; s < 6; ++s)
#pragma unroll
        for (int j = 0; j < 4; ++j) {
          const int k = 32 * s + 8 * g4 + 2 * j;
          af[s][j] = (unsigned)wl[k * 80] | ((unsigned)wl[(k + 1) * 80] << 16);
        }
    }
    const int bb = tid / 96, dp = tid % 96;
    const int oo = WS_O + ((h * 4 + bb) * 192 + 2 * dp) * 16;
    u32x4 a0, a1;
    for (unsigned spins = 0; spins < SPIN_CAP; ++spins) {
      asm volatile("" ::: "memory");
      a0 = ld4<AUX_SC1>(rws, oo, 0); a1 = ld4<AUX_SC1>(rws, oo + 16, 0);
      if (__all(a0[1] == tag && a0[3] == tag && a1[1] == tag && a1[3] == tag)) break;
      __builtin_amdgcn_s_sleep(1);
    }
    ((LAS unsigned*)(L + L_OB))[tid] =
        cvtpk(__uint_as_float(a0[0]) + __uint_as_float(a0[2]), __uint_as_float(a1[0]) + __uint_as_float(a1[2]));
    LAS unsigned* cnt = (LAS unsigned*)(L + L_MISC) + 1;
    asm volatile("s_waitcnt lgkmcnt(0)" ::: "memory");
    if (lane == 0) __hip_atomic_fetch_add(cnt, 1u, __ATOMIC_RELAXED, __HIP_MEMORY_SCOPE_WORKGROUP);
    if (wave < 5) {
      for (unsigned spins = 0; spins < SPIN_CAP; ++spins) {
        if (__hip_atomic_load(cnt, __ATOMIC_RELAXED, __HIP_MEMORY_SCOPE_WORKGROUP) >= 6u) break;
      }
      asm volatile("" ::: "memory");
      f32x4 y = {0.f, 0.f, 0.f, 0.f};
#pragma unroll
      for (int s = 0; s < 6; ++s) {
        const u32x4 bfr = *(const LAS u32x4*)(L + L_OB + (lane & 3) * 384 + (32 * s + 8 * g4) * 2);
        y = __builtin_amdgcn_mfma_f32_16x16x32_bf16(__builtin_bit_cast(bf16x8, af[s]), __builtin_bit_cast(bf16x8, bfr), y, 0, 0, 0);
      }
      if (n16 < 4) {
        const int n = 16 * wave + 4 * g4, q = n / 20, ix = n % 20;
        const int o = WS_Y + ((((bs * 4 + n16) * 4 + q) * 32 + h) * 20 + ix) * 8;
        __builtin_amdgcn_raw_buffer_store_b128(u32x4{__float_as_uint(y[0]), tag, __float_as_uint(y[1]), tag}, rws, o, 0, AUX_SC1);
        __builtin_amdgcn_raw_buffer_store_b128(u32x4{__float_as_uint(y[2]), tag, __float_as_uint(y[3]), tag}, rws, o + 16, 0, AUX_SC1);
      }
    }
    STAMP(11);
  } else if (wave == 7 && h < 16) {
    const int fb = h & 3, fq = h >> 2;
    float gs = 0.f;
    {
      const rsrc_t rg = mk(p.gw, DM * 2);
#pragma unroll
      for (int i = 0; i < 5; ++i) {
        const unsigned xv = __builtin_amdgcn_raw_buffer_load_b32(rx, (fb * DM + 2 * lane + 128 * i) * 2, 0, 0);
        const unsigned gv = __builtin_amdgcn_raw_buffer_load_b32(rg, (2 * lane + 128 * i) * 2, 0, 0);
        gs += rbf(bflo(xv) * bflo(gv)) + rbf(bfhi(xv) * bfhi(gv));
      }
      gs = wave_sum(gs);
    }
    const float gate = rbf(1.f / (1.f + __expf(-rbf(gs))));
    const int hh = lane & 31, e2 = lane >> 5;
    float fs[10], as[10];
    {
      const int o = hh < 8 ? WS_YF + ((((bs * 4 + fb) * 4 + fq) * 8 + hh) * 20 + 10 * e2) * 8 : oob;
      for (unsigned spins = 0; spins < SPIN_CAP; ++spins) {
        asm volatile("" ::: "memory");
        bool ok = true;
#pragma unroll
        for (int i = 0; i < 5; ++i) {
          const u32x4 v = ld4<AUX_SC1>(rws, o + 16 * i, 0);
          ok = ok && v[1] == tag && v[3] == tag;
          fs[2 * i] = __uint_as_float(v[0]); fs[2 * i + 1] = __uint_as_float(v[2]);
        }
        if (__all(ok || hh >= 8)) break;
        __builtin_amdgcn_s_sleep(1);
      }
#pragma unroll
      for (int i = 0; i < 10; ++i) { float t = row16_sum(fs[i]); fs[i] = swap16_add(t, t); }
    }
    {
      const int o = WS_Y + ((((bs * 4 + fb) * 4 + fq) * 32 + hh) * 20 + 10 * e2) * 8;
      for (unsigned spins = 0; spins < SPIN_CAP; ++spins) {
        asm volatile("" ::: "memory");
        bool ok = true;
#pragma unroll
        for (int i = 0; i < 5; ++i) {
          const u32x4 v = ld4<AUX_SC1>(rws, o + 16 * i, 0);
          ok = ok && v[1] == tag && v[3] == tag;
          as[2 * i] = __uint_as_float(v[0]); as[2 * i + 1] = __uint_as_float(v[2]);
        }
        if (__all(ok)) break;
        __builtin_amdgcn_s_sleep(1);
      }
#pragma unroll
      for (int i = 0; i < 10; ++i) { float t = row16_sum(as[i]); as[i] = swap16_add(t, t); }
    }
    float av = as[0], fv = fs[0];
#pragma unroll
    for (int i = 1; i < 10; ++i) { av = hh == i ? as[i] : av; fv = hh == i ? fs[i] : fv; }
    const float res = rbf(gate * rbf(av)) + rbf(rbf(1.f - gate) * rbf(fv));
    if (hh < 10) p.out[fb * DM + 80 * bs + 20 * fq + 10 * e2 + hh] = (unsigned short)(cvtpk(res, res) & 0xffffu);
    if (w == 0 && lane == 0) __builtin_amdgcn_raw_buffer_store_b32(tag, rws, WS_E, 0, AUX_SC1);
    STAMP(12);
  }
  {
    const int so = WS_END + 4096;
    const rsrc_t rs2 = mk(p.ws, WS_END + 4096 + 256 * 16);
    if (tid == 0) __builtin_amdgcn_raw_buffer_store_b128(u32x4{(unsigned)tstart_, tag, (unsigned)(tstart_ >> 32), tag}, rs2, so + w * 16, 0, AUX_SC1);
    unsigned lo = 0;
    unsigned mn = 0xffffffffu;
    for (unsigned spins = 0; spins < SPIN_CAP; ++spins) {
      asm volatile("" ::: "memory");
      bool ok = true; mn = 0xffffffffu;
      for (int i = 0; i < 4; ++i) { const u32x4 v = ld4<AUX_SC1>(rs2, so + (lane + 64 * i) * 16, 0); ok = ok && v[1] == tag && v[3] == tag; const unsigned d = (unsigned)tstart_ - v[0] + 100000u; mn = d > mn && mn != 0xffffffffu ? mn : mn; lo = (i == 0) ? d : (d > lo ? d : lo); }
      if (__all(ok)) break;
      __builtin_amdgcn_s_sleep(8);
    }
    float lf = wave_max((float)lo);
    const unsigned long long earliest = tstart_ + 100000ull - (unsigned long long)lf;
    while (__builtin_amdgcn_s_memrealtime() - earliest < 20000ull) __builtin_amdgcn_s_sleep(4);
  }
  STAMP_FLUSH;
}

}

extern "C" void kernel_launch(void* const* d_in, const int* in_sizes, int n_in, void* d_out, int out_size, void* d_ws,
                              size_t ws_size, hipStream_t stream) {
  Params p;
  p.x = (const unsigned short*)d_in[0];
  p.kc = (const unsigned short*)d_in[1];
  p.vc = (const unsigned short*)d_in[2];
  p.wq = (const unsigned short*)d_in[3];
  p.wo = (const unsigned short*)d_in[4];
  p.w1 = (const unsigned short*)d_in[6];
  p.w3 = (const unsigned short*)d_in[7];
  p.w2 = (const unsigned short*)d_in[8];
  p.gw = (const unsigned short*)d_in[9];
  p.out = (unsigned short*)d_out;
  p.ws = (unsigned char*)d_ws;
  (void)hipFuncSetAttribute((const void*)fused_decode, hipFuncAttributeMaxDynamicSharedMemorySize, L_END);
  fused_decode<<<256, NTHR, L_END, stream>>>(p);
}
```

```cpp
#include <hip/hip_runtime.h>
#include <stdint.h>

namespace {

typedef unsigned u32x4 __attribute__((ext_vector_type(4)));
typedef unsigned u32x2 __attribute__((ext_vector_type(2)));
typedef float f32x4 __attribute__((ext_vector_type(4)));
typedef short s16x4 __attribute__((ext_vector_type(4)));
typedef __bf16 bf16x2 __attribute__((ext_vector_type(2)));
typedef __bf16 bf16x8 __attribute__((ext_vector_type(8)));
#define LAS __attribute__((address_space(3)))

constexpr int DM = 640, NH = 32, SK = 8192, DH = 192, FF = 1536;
constexpr int NTHR = 512;
constexpr unsigned POLL_AT = 400;
constexpr unsigned PACE_0 = 750, PACE_T = 12000;
constexpr unsigned ADAPT_0 = 120, ADAPT_B = 40, ADAPT_MAX = 900;
constexpr unsigned TIGHT_AT = PACE_T - 100;
constexpr int SKEW = 2;
constexpr unsigned KV_BH_BYTES = 8192u * 384u;
constexpr int AUX_NT = 2, AUX_SC1 = 16;
constexpr int RSRC_W3 = 0x00020000;
constexpr int OOB = 0x40000000;

constexpr int WS_E = 0;
constexpr int WS_XP = 1024;
constexpr int WS_SL = 4096;
constexpr int WS_RP = WS_SL + 2048;
constexpr int WS_ML = 8192;
constexpr int WS_O = WS_ML + 128 * 16 * 16;
constexpr int WS_Y = WS_O + 32 * 4 * 192 * 16;
constexpr int WS_YF = WS_Y + 32 * 4 * 640 * 8;
constexpr int WS_QP = WS_YF + 64 * 4 * 640 * 8;
constexpr int WS_FP = WS_QP + 32 * 4 * 8 * 192 * 8;
constexpr int WS_END = WS_FP + 2 * 8 * 8 * 4 * 192 * 8;

constexpr int L_SC = 0;
constexpr int L_RED = L_SC + 8 * 576 * 4;
constexpr int L_QB = L_RED + 8 * 192 * 4;
constexpr int L_OB = L_QB + 384;
constexpr int L_HB = L_OB + 4 * 384;
constexpr int L_MISC = L_HB + 256;
constexpr int L_WQ = L_MISC + 64;
constexpr int L_XA = L_WQ + 30 * 1024;
constexpr int L_XF = L_WQ + 31 * 1024;
constexpr int L_EP = L_WQ + 32 * 1024;
constexpr int L_WO = L_WQ + 33 * 1024;
constexpr int L_WF = L_WO + 32 * 1024;
constexpr int L_END = L_WF + 32 * 1024;
static_assert(L_END > 80 * 1024 && L_END <= 160 * 1024, "one workgroup per CU");

constexpr unsigned SPIN_CAP = 1u << 17;

__device__ __forceinline__ float bflo(unsigned u) { return __uint_as_float(u << 16); }
__device__ __forceinline__ float bfhi(unsigned u) { return __uint_as_float(u & 0xffff0000u); }
__device__ __forceinline__ unsigned cvtpk(float lo, float hi) {
  bf16x2 r = {(__bf16)lo, (__bf16)hi};
  return __builtin_bit_cast(unsigned, r);
}
__device__ __forceinline__ float rbf(float f) { return bflo(cvtpk(f, f)); }
__device__ __forceinline__ float dot2(unsigned a, unsigned b, float c) {
  return __builtin_amdgcn_fdot2_f32_bf16(__builtin_bit_cast(bf16x2, a), __builtin_bit_cast(bf16x2, b), c, false);
}
template <int CTRL> __device__ __forceinline__ float dpp(float x) {
  return __builtin_bit_cast(float, __builtin_amdgcn_mov_dpp(__builtin_bit_cast(int, x), CTRL, 0xf, 0xf, true));
}
constexpr int XOR1 = 0xB1, XOR2 = 0x4E, HMIR = 0x141, MIR = 0x140, ROR8 = 0x128;
__device__ __forceinline__ float swap32_add(float a, float b) {
  auto s = __builtin_amdgcn_permlane32_swap(__float_as_uint(a), __float_as_uint(b), false, false);
  return __uint_as_float(s[0]) + __uint_as_float(s[1]);
}
__device__ __forceinline__ float swap16_add(float a, float b) {
  auto s = __builtin_amdgcn_permlane16_swap(__float_as_uint(a), __float_as_uint(b), false, false);
  return __uint_as_float(s[0]) + __uint_as_float(s[1]);
}
__device__ __forceinline__ float swap32_max(float a) {
  auto s = __builtin_amdgcn_permlane32_swap(__float_as_uint(a), __float_as_uint(a), false, false);
  return fmaxf(__uint_as_float(s[0]), __uint_as_float(s[1]));
}
__device__ __forceinline__ float swap16_max(float a) {
  auto s = __builtin_amdgcn_permlane16_swap(__float_as_uint(a), __float_as_uint(a), false, false);
  return fmaxf(__uint_as_float(s[0]), __uint_as_float(s[1]));
}
__device__ __forceinline__ float row16_sum(float x) { x += dpp<XOR1>(x); x += dpp<XOR2>(x); x += dpp<HMIR>(x); x += dpp<MIR>(x); return x; }
__device__ __forceinline__ float row16_max(float x) {
  x = fmaxf(x, dpp<XOR1>(x)); x = fmaxf(x, dpp<XOR2>(x)); x = fmaxf(x, dpp<HMIR>(x)); x = fmaxf(x, dpp<MIR>(x)); return x;
}
__device__ __forceinline__ float wave_sum(float x) { x = row16_sum(x); x = swap16_add(x, x); return swap32_add(x, x); }
__device__ __forceinline__ float wave_max(float x) { x = row16_max(x); x = swap16_max(x); return swap32_max(x); }
__device__ __forceinline__ float sel4(f32x4 a, int g) { return g == 0 ? a[0] : g == 1 ? a[1] : g == 2 ? a[2] : a[3]; }

typedef __amdgpu_buffer_rsrc_t rsrc_t;
__device__ __forceinline__ rsrc_t mk(const void* p, unsigned bytes) { return __builtin_amdgcn_make_buffer_rsrc((void*)p, 0, bytes, RSRC_W3); }
template <int AUX> __device__ __forceinline__ u32x4 ld4(rsrc_t r, int voff, int soff) { return __builtin_amdgcn_raw_buffer_load_b128(r, voff, soff, AUX); }
__device__ __forceinline__ s16x4 trrd(LAS unsigned char* a) { return __builtin_amdgcn_ds_read_tr16_b64_v4i16((LAS s16x4*)a); }
__device__ __forceinline__ bf16x8 frag8(s16x4 a, s16x4 b) {
  const u32x2 x = __builtin_bit_cast(u32x2, a), y = __builtin_bit_cast(u32x2, b);
  return __builtin_bit_cast(bf16x8, u32x4{x[0], x[1], y[0], y[1]});
}
__device__ __forceinline__ void dma16(rsrc_t r, int voff, int soff, int dst) {
  unsigned keep;
  asm volatile("s_nop 4\n\ts_mov_b32 %0, m0\n\ts_mov_b32 m0, %4\n\ts_nop 0\n\tbuffer_load_dwordx4 %1, %2, %3 offen nt lds\n\ts_mov_b32 m0, %0"
               : "=&s"(keep) : "v"(voff), "s"(r), "s"(soff), "s"(dst) : "memory");
}
__device__ __forceinline__ void dma16_sc1(rsrc_t r, int voff, int soff, int dst) {
  unsigned keep;
  asm volatile("s_nop 4\n\ts_mov_b32 %0, m0\n\ts_mov_b32 m0, %4\n\ts_nop 0\n\tbuffer_load_dwordx4 %1, %2, %3 offen sc1 lds\n\ts_mov_b32 m0, %0"
               : "=&s"(keep) : "v"(voff), "s"(r), "s"(soff), "s"(dst) : "memory");
}

__device__ __forceinline__ void reduce24(const float (&a)[24], float (&o)[3], int lane) {
  float t[12], u[6];
#pragma unroll
  for (int i = 0; i < 12; ++i) t[i] = swap32_add(a[i], a[i + 12]);
#pragma unroll
  for (int i = 0; i < 6; ++i) u[i] = swap16_add(t[i], t[i + 6]);
#pragma unroll
  for (int i = 0; i < 3; ++i) {
    const float x = u[i] + dpp<ROR8>(u[i]), y = u[i + 3] + dpp<ROR8>(u[i + 3]);
    o[i] = (lane & 8) ? y : x;
  }
}

struct Params {
  const unsigned short* x;
  const unsigned short* kc;
  const unsigned short* vc;
  const unsigned short* wq;
  const unsigned short* wo;
  const unsigned short* w1;
  const unsigned short* w3;
  const unsigned short* w2;
  const unsigned short* gw;
  unsigned short* out;
  unsigned char* ws;
};

__global__ void __launch_bounds__(NTHR, 2) fused_decode(Params p) {
  extern __shared__ __attribute__((aligned(16))) unsigned char lds_raw[];
  LAS unsigned char* L = (LAS unsigned char*)lds_raw;
  const int lds0 = (int)(unsigned)(uintptr_t)L;
  const int tid = threadIdx.x, lane = tid & 63;
  const int wave = __builtin_amdgcn_readfirstlane(tid >> 6);
  const int r = lane >> 3, c = lane & 7;
  const int g4 = lane >> 4, n16 = lane & 15;
  const int w = blockIdx.x, mem = w & 1, h = (w >> 1) & 31, b = w >> 6, j = 2 * b + mem;
  const int ph = w & 7, role = (w >> 3) & 3, jf = w >> 5;
  const unsigned long long t_start = __builtin_amdgcn_s_memrealtime();
  bool tight = false;
  unsigned tight_at = TIGHT_AT;
  auto poll_pause = [&]() __attribute__((always_inline)) {
    if (!tight) {
      tight = (unsigned)(__builtin_amdgcn_s_memrealtime() - t_start) >= tight_at;
      if (!tight) __builtin_amdgcn_s_sleep(24);
    }
  };

  const size_t kvbase = (size_t)(b * NH + h) * SK * DH;
  {
    const rsrc_t rq = mk(p.wq + (size_t)(80 * j) * (NH * DH) + h * DH, ((DM - 80 * j) * NH * DH - h * DH) * 2);
    const int vq = r * (NH * DH * 2) + c * 16;
#pragma unroll
    for (int u = 0; u < 3; ++u) {
      const int pc = u * 8 + wave, t = pc / 3, i = pc - 3 * t;
      dma16(rq, vq, t * (8 * NH * DH * 2) + i * 128, lds0 + L_WQ + pc * 1024);
    }
    if (wave < 6) {
      const int pc = 24 + wave, t = pc / 3, i = pc - 3 * t;
      dma16(rq, vq, t * (8 * NH * DH * 2) + i * 128, lds0 + L_WQ + pc * 1024);
    } else {
      dma16(mk(p.x, 4 * DM * 2), lane < 40 ? (lane / 10) * (DM * 2) + (lane % 10) * 16 : OOB, 160 * (wave == 6 ? j : jf), lds0 + (wave == 6 ? L_XA : L_XF));
      if (wave == 7) dma16_sc1(mk(p.ws, 16), lane * 16, 0, lds0 + L_EP);
    }
  }
  __builtin_amdgcn_s_barrier();
  const rsrc_t rws = mk(p.ws, WS_END);
  const rsrc_t rx = mk(p.x, 4 * DM * 2);
  const rsrc_t rk = mk(p.kc + kvbase, KV_BH_BYTES);
  const int voff = r * 384 + c * 16;
  const int tile0 = (mem ? 1023 - wave : wave) * 3072, tstep = mem ? -24576 : 24576;
  u32x4 ring[4][3];
#pragma unroll
  for (int i = 0; i < 3; ++i) { ring[0][i] = ld4<AUX_NT>(rk, voff + i * 128, tile0); __builtin_amdgcn_sched_barrier(0); }
  if (tid < 4) ((LAS unsigned*)(L + L_MISC))[tid] = 0u;
  if (tid >= 64 && tid < 128) ((LAS unsigned*)(L + L_HB))[tid - 64] = 0u;
  asm volatile("s_waitcnt vmcnt(3) lgkmcnt(0)" ::: "memory");
  __builtin_amdgcn_s_barrier();
  asm volatile("" ::: "memory");
  const unsigned epoch = __builtin_amdgcn_readfirstlane(*(const LAS unsigned*)(L + L_EP));
  const unsigned tag = epoch + 1u != 0u ? epoch + 1u : 1u;
  auto load_xa = [&](u32x2 (&xa)[5], int img) __attribute__((always_inline)) {
#pragma unroll
    for (int s = 0; s < 5; ++s) xa[s] = *(const LAS u32x2*)(L + img + (lane & 3) * 160 + (16 * s + 4 * g4) * 2);
  };
  const unsigned xpar = (unsigned)__builtin_amdgcn_s_getreg(20 | (3 << 11)) & 1u;
  if (tid == 448) __builtin_amdgcn_raw_buffer_store_b64(u32x2{xpar, tag}, rws, WS_XP + w * 8, 0, AUX_SC1);
  if (wave < 6) {
    u32x2 xa[5];
    load_xa(xa, L_XA);
#pragma unroll
    for (int t = 0; t < 2; ++t) {
      const int nt = 2 * wave + t;
      LAS unsigned char* a = L + L_WQ + (3 * (g4 >> 1) + (nt >> 2)) * 1024 + (4 * (g4 & 1) + (n16 >> 2)) * 128 + (nt & 3) * 32 + (lane & 3) * 8;
      s16x4 bq[5];
#pragma unroll
      for (int s = 0; s < 5; ++s) bq[s] = trrd(a + s * 6144);
      f32x4 acc = {0.f, 0.f, 0.f, 0.f};
#pragma unroll
      for (int s = 0; s < 5; ++s) acc = __builtin_amdgcn_mfma_f32_16x16x16bf16_1k(__builtin_bit_cast(s16x4, xa[s]), bq[s], acc, 0, 0, 0);
      __builtin_amdgcn_raw_buffer_store_b64(u32x2{__float_as_uint(sel4(acc, g4)), tag}, rws, WS_QP + (((h * 4 + g4) * 8 + j) * 192 + 16 * nt + n16) * 8, 0, AUX_SC1);
    }
  }
  __builtin_amdgcn_sched_barrier(0);
  __builtin_amdgcn_s_barrier();
#pragma unroll
  for (int i = 0; i < 3; ++i) { ring[1][i] = ld4<AUX_NT>(rk, voff + i * 128, tile0 + tstep); __builtin_amdgcn_sched_barrier(0); }
  LAS unsigned* misc = (LAS unsigned*)(L + L_MISC);
  const int qt = (wave - 6) * 64 + lane;
  const int qo = (wave >= 6 && qt < 96) ? WS_QP + ((h * 4 + b) * 8 * 192 + 2 * qt) * 8 : OOB;
  if (wave >= 6) {
    while ((unsigned)(__builtin_amdgcn_s_memrealtime() - t_start) < POLL_AT) __builtin_amdgcn_s_sleep(1);
    u32x4 pv[8];
    u32x2 xp;
#pragma unroll
    for (int i = 0; i < 8; ++i) pv[i] = ld4<AUX_SC1>(rws, qo + i * (192 * 8), 0);
    xp = __builtin_amdgcn_raw_buffer_load_b64(rws, WS_XP + (w ^ 1) * 8, 0, AUX_SC1);
    __builtin_amdgcn_sched_barrier(0);
    if (lane == 0) __hip_atomic_fetch_add(misc + 1, 1u, __ATOMIC_RELAXED, __HIP_MEMORY_SCOPE_WORKGROUP);
    bool ok = xp[1] == tag;
#pragma unroll
    for (int i = 0; i < 8; ++i) ok = ok & (((pv[i][1] == tag) & (pv[i][3] == tag)) | (qt >= 96));
    if (!__all(ok)) {
      for (unsigned spins = 0; spins < SPIN_CAP; ++spins) {
        asm volatile("" ::: "memory");
        xp = __builtin_amdgcn_raw_buffer_load_b64(rws, WS_XP + (w ^ 1) * 8, 0, AUX_SC1);
        ok = xp[1] == tag;
#pragma unroll
        for (int i = 0; i < 8; ++i) { pv[i] = ld4<AUX_SC1>(rws, qo + i * (192 * 8), 0); ok = ok & (((pv[i][1] == tag) & (pv[i][3] == tag)) | (qt >= 96)); }
        if (__all(ok)) break;
      }
    }
    float q0 = 0.f, q1 = 0.f;
#pragma unroll
    for (int i = 0; i < 8; ++i) { q0 += __uint_as_float(pv[i][0]); q1 += __uint_as_float(pv[i][2]); }
    asm volatile("" : "+v"(q0), "+v"(q1), "+v"(xp));
    if (qt < 96) ((LAS unsigned*)(L + L_QB))[qt] = cvtpk(q0, q1);
    if (tid == 384) misc[2] = xp[0];
  } else {
    for (unsigned spins = 0; spins < SPIN_CAP; ++spins) {
      if (__hip_atomic_load(misc + 1, __ATOMIC_RELAXED, __HIP_MEMORY_SCOPE_WORKGROUP) >= 2u) break;
      __builtin_amdgcn_s_sleep(1);
    }
  }
  __builtin_amdgcn_sched_barrier(0);
#pragma unroll
  for (int u = 2; u < 4; ++u) {
#pragma unroll
    for (int i = 0; i < 3; ++i) { ring[u][i] = ld4<AUX_NT>(rk, voff + i * 128, tile0 + u * tstep); __builtin_amdgcn_sched_barrier(0); }
  }
  asm volatile("s_waitcnt lgkmcnt(0)" ::: "memory");
  __builtin_amdgcn_s_barrier();
  asm volatile("" ::: "memory");
  u32x4 qv[3];
#pragma unroll
  for (int i = 0; i < 3; ++i) qv[i] = *(const LAS u32x4*)(L + L_QB + (c + 8 * i) * 16);
  const unsigned opar = __builtin_amdgcn_readfirstlane(misc[2]);
  const int nt = 64 + (xpar == opar ? 0 : xpar ? -SKEW : SKEW);
  const int ni = (nt + 7) >> 3;
  const int nn = 8 * ni - nt;
  auto tile_of = [&](int it, int u) __attribute__((always_inline)) { return (u == 7 && it < nn) ? -1 : it * 8 + u - min(it, nn); };
  auto done_by = [&](int it, int u) __attribute__((always_inline)) { return it * 8 + u - min(u == 8 ? it + 1 : it, nn); };

  unsigned plan_t = PACE_0, plan_end = PACE_T;
  int plan_d = 0;
  auto plan_due = [&](int done) __attribute__((always_inline)) { return plan_t + (unsigned)(done - plan_d) * (plan_end - plan_t) / (unsigned)(2 * nt - plan_d); };
  auto round_end = [&](int done) __attribute__((always_inline)) {
    if (wave == 0) {
      const unsigned due = plan_due(done);
      while ((unsigned)(__builtin_amdgcn_s_memrealtime() - t_start) < due) __builtin_amdgcn_s_sleep(1);
    }
    __builtin_amdgcn_s_barrier();
  };

  LAS float* sc = (LAS float*)(L + L_SC) + wave * 576;
  float mx = -INFINITY;
  u32x4 wov[4], wfv[5];
  {
    const rsrc_t ro = mk(p.wo + (size_t)h * DH * DM + j * 80, (DH * DM - j * 80) * 2);
#pragma unroll
    for (int u = 0; u < 4; ++u) {
      const int id = (u * 8 + wave) * 64 + lane;
      wov[u] = ld4<AUX_NT>(ro, id < 1920 ? (id / 10) * (DM * 2) + (id % 10) * 16 : OOB, 0);
      __builtin_amdgcn_sched_barrier(0);
    }
    const bool s1 = (role == 1 || role == 2) && wave < 6;
    const int frow = 80 * jf + 40 * (wave / 3), fcol = ph * 192 + 64 * (wave % 3);
    const unsigned short* fb = role == 0 ? p.w2 + (size_t)(ph * DH + 24 * jf) * DM : (role == 1 ? p.w1 : p.w3) + (size_t)frow * FF + fcol;
    const rsrc_t rf = mk(fb, role == 0 ? 24 * DM * 2 : s1 ? ((DM - frow) * FF - fcol) * 2 : 0);
    const int fv = role == 0 ? lane * 16 : r * (FF * 2) + c * 16;
    const int fs0 = role == 0 ? wave * 1024 : 0, fs1 = role == 0 ? 8192 : 8 * FF * 2;
#pragma unroll
    for (int u = 0; u < 5; ++u) { wfv[u] = ld4<AUX_NT>(rf, fv, fs0 + u * fs1); __builtin_amdgcn_sched_barrier(0); }
  }
  auto kiter = [&](int it) __attribute__((always_inline)) {
    float sreg = 0.f;
#pragma unroll
    for (int u = 0; u < 8; ++u) {
      const int slot = u & 3;
      float d0 = 0.f, d1 = 0.f, d2 = 0.f;
#pragma unroll
      for (int j = 0; j < 4; ++j) {
        d0 = dot2(ring[slot][0][j], qv[0][j], d0);
        d1 = dot2(ring[slot][1][j], qv[1][j], d1);
        d2 = dot2(ring[slot][2][j], qv[2][j], d2);
      }
      asm volatile("" : "+v"(d0), "+v"(d1), "+v"(d2));
      __builtin_amdgcn_sched_barrier(0);
      {
        const bool isv = (u >= 4) && (it == ni - 1);
        const int tn = u < 4 ? tile_of(it, u + 4) : isv ? tile_of(0, u - 4) : tile_of(it + 1, u - 4);
        const rsrc_t rr = mk(isv ? (const void*)(p.vc + kvbase) : (const void*)(p.kc + kvbase), tn >= 0 ? KV_BH_BYTES : 0u);
#pragma unroll
        for (int i = 0; i < 3; ++i) ring[slot][i] = ld4<AUX_NT>(rr, voff + i * 128, tile0 + tn * tstep);
      }
      __builtin_amdgcn_sched_barrier(0);
      float d = (d0 + d1) + d2;
      d += dpp<XOR1>(d); d += dpp<XOR2>(d); d += dpp<HMIR>(d);
      float s = rbf(rbf(d) * 0.07216878364870322f) * 1.4426950408889634f;
      if (u == 7) s = it < nn ? -INFINITY : s;
      sreg = (c == u) ? s : sreg;
      if (u == 3) round_end(done_by(it, 4));
    }
    mx = fmaxf(mx, sreg);
    sc[it * 64 + lane] = sreg;
  };
  kiter(0);
#pragma unroll
  for (int u = 0; u < 4; ++u) *(LAS u32x4*)(L + L_WO + (u * 8 + wave) * 1024 + lane * 16) = wov[u];
  {
    const int fl = role == 0 ? wave * 1024 : (wave % 3) * 10240 + (wave / 3) * 5120, fstep = role == 0 ? 8192 : 1024;
#pragma unroll
    for (int u = 0; u < 5; ++u)
      if (role == 0 ? u < 4 : wave < 6) *(LAS u32x4*)(L + L_WF + fl + u * fstep + lane * 16) = wfv[u];
  }
  asm volatile("s_waitcnt lgkmcnt(0)" ::: "memory");
  round_end(done_by(0, 8));
  const int fp_off = WS_FP + ((ph * 8 + jf) * 8 + wave) * (192 * 8) + lane * 16, fp_off2 = lane < 32 ? fp_off + 1024 : OOB;
  u32x4 fp0 = {0u, 0u, 0u, 0u}, fp1 = {0u, 0u, 0u, 0u};
  u32x4 sl0 = {0u, 0u, 0u, 0u}, sl1 = {0u, 0u, 0u, 0u};
  for (int it = 1; it < ni; ++it) {
    if (it == 1 && (role == 1 || role == 2) && wave < 6) {
      u32x2 xa[5];
      load_xa(xa, L_XF);
#pragma unroll
      for (int t = 0; t < 2; ++t) {
        const int nt2 = 2 * wave + t;
        LAS unsigned char* a = L + L_WF + (nt2 >> 2) * 10240 + (4 * g4 + (n16 >> 2)) * 128 + (nt2 & 3) * 32 + (lane & 3) * 8;
        s16x4 bq[5];
#pragma unroll
        for (int s = 0; s < 5; ++s) bq[s] = trrd(a + s * 2048);
        f32x4 acc = {0.f, 0.f, 0.f, 0.f};
#pragma unroll
        for (int s = 0; s < 5; ++s) acc = __builtin_amdgcn_mfma_f32_16x16x16bf16_1k(__builtin_bit_cast(s16x4, xa[s]), bq[s], acc, 0, 0, 0);
        const int col = 32 * wave + 16 * t + n16;
        __builtin_amdgcn_raw_buffer_store_b64(u32x2{__float_as_uint(sel4(acc, g4)), tag}, rws,
                                              WS_FP + (((((ph * 8 + col / 24) * 8 + jf) * 2 + (role - 1)) * 4 + g4) * 24 + col % 24) * 8, 0, AUX_SC1);
      }
    }
    if (role == 0) {
      if (it == 4) {
        u32x4 f0 = fp0, f1 = fp1;
        if (!__all((f0[1] == tag) & (f0[3] == tag) & (((f1[1] == tag) & (f1[3] == tag)) | (lane >= 32)))) {
          for (unsigned spins = 0; spins < SPIN_CAP; ++spins) {
            asm volatile("" ::: "memory");
            f0 = ld4<AUX_SC1>(rws, fp_off, 0);
            f1 = ld4<AUX_SC1>(rws, fp_off2, 0);
            if (__all((f0[1] == tag) & (f0[3] == tag) & (((f1[1] == tag) & (f1[3] == tag)) | (lane >= 32)))) break;
          }
          asm volatile("" : "+v"(f0), "+v"(f1));
        }
        LAS float* fs = (LAS float*)(L + L_RED) + wave * 192;
        *(LAS u32x2*)(fs + 2 * lane) = u32x2{f0[0], f0[2]};
        if (lane < 32) *(LAS u32x2*)(fs + 128 + 2 * lane) = u32x2{f1[0], f1[2]};
      } else if (it == 5) {
        if (tid < 96) {
          const LAS float* fs = (const LAS float*)(L + L_RED) + tid;
          float gg = 0.f, uu = 0.f;
#pragma unroll
          for (int v = 0; v < 8; ++v) { gg += fs[v * 192]; uu += fs[v * 192 + 96]; }
          gg = rbf(gg); uu = rbf(uu);
          const float hv = rbf(gg / (1.f + __expf(-gg))) * uu;
          ((LAS unsigned short*)(L + L_HB))[(tid / 24) * 32 + tid % 24] = (unsigned short)(cvtpk(hv, hv) & 0xffffu);
        }
      } else if (it == 6) {
        const u32x2 h0 = *(const LAS u32x2*)(L + L_HB + (lane & 3) * 64 + g4 * 8), h1 = *(const LAS u32x2*)(L + L_HB + (lane & 3) * 64 + 32 + g4 * 8);
        LAS unsigned char* a0 = L + L_WF + (4 * g4 + (n16 >> 2)) * 1280 + (80 * wave + 4 * (lane & 3)) * 2;
        LAS unsigned char* a1 = L + L_WF + (16 + 4 * (g4 & 1) + (n16 >> 2)) * 1280 + (80 * wave + 4 * (lane & 3)) * 2;
#pragma unroll
        for (int t = 0; t < 5; ++t) {
          f32x4 acc = {0.f, 0.f, 0.f, 0.f};
          acc = __builtin_amdgcn_mfma_f32_16x16x16bf16_1k(__builtin_bit_cast(s16x4, h0), trrd(a0 + t * 32), acc, 0, 0, 0);
          acc = __builtin_amdgcn_mfma_f32_16x16x16bf16_1k(__builtin_bit_cast(s16x4, h1), trrd(a1 + t * 32), acc, 0, 0, 0);
          __builtin_amdgcn_raw_buffer_store_b64(u32x2{__float_as_uint(sel4(acc, g4)), tag}, rws,
                                                WS_YF + (((ph * 8 + jf) * 4 + g4) * 640 + 80 * wave + 16 * t + n16) * 8, 0, AUX_SC1);
        }
      }
    }
    {
      if (it == 6 && wave == 0) {
        const unsigned due = plan_due(done_by(6, 0)), now = (unsigned)(__builtin_amdgcn_s_memrealtime() - t_start);
        if (lane == 0) __builtin_amdgcn_raw_buffer_store_b64(u32x2{now > due ? now - due : 0u, tag}, rws, WS_SL + w * 8, 0, AUX_SC1);
      }
      const rsrc_t rsl = mk(p.ws, (it == ni - 1 && wave == 1 && w < 8) ? WS_END : 0);
      sl0 = ld4<AUX_SC1>(rsl, WS_SL + lane * 32, 0);
      sl1 = ld4<AUX_SC1>(rsl, WS_SL + lane * 32 + 16, 0);
    }
    {
      const rsrc_t rfp = mk(p.ws, (role == 0 && it == 3) ? WS_END : 0);
      fp0 = ld4<AUX_SC1>(rfp, fp_off, 0);
      fp1 = ld4<AUX_SC1>(rfp, fp_off2, 0);
      __builtin_amdgcn_sched_barrier(0);
    }
    kiter(it);
    asm volatile("s_waitcnt lgkmcnt(0)" ::: "memory");
    round_end(done_by(it, 8));
  }
  const float mw = wave_max(mx);
  {
    float l = 0.f;
#pragma unroll
    for (int it = 0; it < 8; ++it) {
      const float pe = __builtin_amdgcn_exp2f(sc[it * 64 + lane] - mw);
      l += pe;
      sc[it * 64 + lane] = pe;
    }
    if (ni > 8) {
      const float pe = __builtin_amdgcn_exp2f(sc[8 * 64 + lane] - mw);
      l += pe;
      sc[8 * 64 + lane] = pe;
    }
    l = wave_sum(l);
    if (lane == 0) {
      const u32x4 gr = {__float_as_uint(mw), tag, __float_as_uint(l), tag};
      __builtin_amdgcn_raw_buffer_store_b128(gr, rws, WS_ML + ((b * NH + h) * 16 + mem * 8 + wave) * 16, 0, AUX_SC1);
    }
  }

  float acc[24];
#pragma unroll
  for (int i = 0; i < 24; ++i) acc[i] = 0.f;
  u32x4 ml = {0u, 0u, 0u, 0u};
  u32x2 rp = {0u, 0u};
  float fnorm = 0.f;
  const int mo = WS_ML + ((b * NH + h) * 16 + n16) * 16;
  for (int it = 0; it < ni; ++it) {
    const bool last = it == ni - 1;
    const f32x4 pa = *(const LAS f32x4*)(sc + it * 64 + r * 8), pb = *(const LAS f32x4*)(sc + it * 64 + r * 8 + 4);
    {
      if (it == 0 && wave == 1 && w < 8) {
        float s = (sl0[1] == tag ? (float)sl0[0] : 800.f) + (sl0[3] == tag ? (float)sl0[2] : 800.f) + (sl1[1] == tag ? (float)sl1[0] : 800.f) + (sl1[3] == tag ? (float)sl1[2] : 800.f);
        s = wave_sum(s);
        if (lane == 0) __builtin_amdgcn_raw_buffer_store_b64(u32x2{(unsigned)(s * (1.f / 256.f)), tag}, rws, WS_RP + w * 128, 0, AUX_SC1);
      }
      if (it == 2 && wave == 0) {
        const unsigned avg = __builtin_amdgcn_readfirstlane(rp[0]), rt = __builtin_amdgcn_readfirstlane(rp[1]);
        if (rt == tag && avg > ADAPT_0) {
          unsigned ext = (avg - ADAPT_0) * ADAPT_B / 16;
          ext = ext > ADAPT_MAX ? ADAPT_MAX : ext;
          const int d = nt + done_by(2, 0);
          plan_t = plan_due(d); plan_d = d; plan_end += ext;
          if (lane == 0) misc[3] = ext;
        }
      }
      rp = __builtin_amdgcn_raw_buffer_load_b64(mk(p.ws, (it == 1 && wave == 0) ? WS_END : 0), WS_RP + (w & 7) * 128, 0, AUX_SC1);
    }
    if (last) {
      if (!__all((ml[1] == tag) & (ml[3] == tag))) {
        for (unsigned spins = 0; spins < SPIN_CAP; ++spins) {
          asm volatile("" ::: "memory");
          ml = ld4<AUX_SC1>(rws, mo, 0);
          if (__all((ml[1] == tag) & (ml[3] == tag))) break;
        }
        asm volatile("" : "+v"(ml));
      }
      const float mi = __uint_as_float(ml[0]), li = __uint_as_float(ml[2]);
      const float M = row16_max(mi);
      fnorm = __builtin_amdgcn_exp2f(mw - M) / row16_sum(li * __builtin_amdgcn_exp2f(mi - M));
    }
    ml = ld4<AUX_SC1>(mk(p.ws, it == ni - 2 ? WS_END : 0), mo, 0);
    __builtin_amdgcn_sched_barrier(0);
#pragma unroll
    for (int u = 0; u < 8; ++u) {
      const int slot = u & 3;
      const float pe = u < 4 ? pa[u] : pb[u - 4];
#pragma unroll
      for (int i = 0; i < 3; ++i)
#pragma unroll
        for (int j = 0; j < 4; ++j) {
          acc[i * 8 + 2 * j] = fmaf(pe, bflo(ring[slot][i][j]), acc[i * 8 + 2 * j]);
          acc[i * 8 + 2 * j + 1] = fmaf(pe, bfhi(ring[slot][i][j]), acc[i * 8 + 2 * j + 1]);
        }
      asm volatile("" : "+v"(acc[0]), "+v"(acc[1]), "+v"(acc[2]), "+v"(acc[3]), "+v"(acc[4]), "+v"(acc[5]), "+v"(acc[6]), "+v"(acc[7]),
                        "+v"(acc[8]), "+v"(acc[9]), "+v"(acc[10]), "+v"(acc[11]), "+v"(acc[12]), "+v"(acc[13]), "+v"(acc[14]), "+v"(acc[15]),
                        "+v"(acc[16]), "+v"(acc[17]), "+v"(acc[18]), "+v"(acc[19]), "+v"(acc[20]), "+v"(acc[21]), "+v"(acc[22]), "+v"(acc[23]));
      __builtin_amdgcn_sched_barrier(0);
      {
        const int tn = u < 4 ? tile_of(it, u + 4) : last ? -1 : tile_of(it + 1, u - 4);
        const rsrc_t rr = mk(p.vc + kvbase, tn >= 0 ? KV_BH_BYTES : 0u);
#pragma unroll
        for (int i = 0; i < 3; ++i) ring[slot][i] = ld4<AUX_NT>(rr, voff + i * 128, tile0 + tn * tstep);
      }
      __builtin_amdgcn_sched_barrier(0);
      if (u == 3 && !last) round_end(nt + done_by(it, 4));
    }
    if (!last) round_end(nt + done_by(it, 8));
  }
  tight_at += __builtin_amdgcn_readfirstlane(misc[3]);
  {
    float o[3];
    reduce24(acc, o, lane);
    const int base = 12 * (lane >> 5) + 6 * ((lane >> 4) & 1) + 3 * ((lane >> 3) & 1);
    LAS float* red = (LAS float*)(L + L_RED) + wave * 192;
#pragma unroll
    for (int i = 0; i < 3; ++i) { const int ai = base + i; red[(c + 8 * (ai >> 3)) * 8 + (ai & 7)] = o[i] * fnorm; }
  }
  __syncthreads();
  if (tid < 192) {
    const LAS float* red = (const LAS float*)(L + L_RED);
    float s = 0.f;
#pragma unroll
    for (int v = 0; v < 8; ++v) s += red[v * 192 + tid];
    __builtin_amdgcn_raw_buffer_store_b64(u32x2{__float_as_uint(s), tag}, rws, WS_O + (((h * 4 + b) * 2 + mem) * 192 + tid) * 8, 0, AUX_SC1);
  }

  if (wave < 6) {
    bf16x8 wf[6];
    if (wave < 5) {
      LAS unsigned char* a = L + L_WO + (8 * g4 + (n16 >> 2)) * 160 + (16 * wave + 4 * (lane & 3)) * 2;
#pragma unroll
      for (int s = 0; s < 6; ++s) wf[s] = frag8(trrd(a + s * 32 * 160), trrd(a + s * 32 * 160 + 4 * 160));
    }
    const int bb = tid / 96, dp = tid % 96;
    const int oo = WS_O + ((h * 4 + bb) * 2 * 192 + 2 * dp) * 8;
    u32x4 a0 = ld4<AUX_SC1>(rws, oo, 0), a1 = ld4<AUX_SC1>(rws, oo + 1536, 0);
    for (unsigned spins = 0; spins < SPIN_CAP; ++spins) {
      const bool k0 = (a0[1] == tag) & (a0[3] == tag), k1 = (a1[1] == tag) & (a1[3] == tag);
      if (__all(k0 & k1)) break;
      poll_pause();
      asm volatile("" ::: "memory");
      const u32x4 b0 = ld4<AUX_SC1>(rws, k0 ? OOB : oo, 0), b1 = ld4<AUX_SC1>(rws, k1 ? OOB : oo + 1536, 0);
      a0 = k0 ? a0 : b0; a1 = k1 ? a1 : b1;
    }
    ((LAS unsigned*)(L + L_OB))[tid] =
        cvtpk(__uint_as_float(a0[0]) + __uint_as_float(a1[0]), __uint_as_float(a0[2]) + __uint_as_float(a1[2]));
    LAS unsigned* cnt = (LAS unsigned*)(L + L_MISC);
    asm volatile("s_waitcnt lgkmcnt(0)" ::: "memory");
    if (lane == 0) __hip_atomic_fetch_add(cnt, 1u, __ATOMIC_RELAXED, __HIP_MEMORY_SCOPE_WORKGROUP);
    if (wave < 5) {
      for (unsigned spins = 0; spins < SPIN_CAP; ++spins) {
        if (__hip_atomic_load(cnt, __ATOMIC_RELAXED, __HIP_MEMORY_SCOPE_WORKGROUP) >= 6u) break;
      }
      asm volatile("" ::: "memory");
      f32x4 y = {0.f, 0.f, 0.f, 0.f};
#pragma unroll
      for (int s = 0; s < 6; ++s) {
        const u32x4 ofr = *(const LAS u32x4*)(L + L_OB + (lane & 3) * 384 + (32 * s + 8 * g4) * 2);
        y = __builtin_amdgcn_mfma_f32_16x16x32_bf16(__builtin_bit_cast(bf16x8, ofr), wf[s], y, 0, 0, 0);
      }
      __builtin_amdgcn_raw_buffer_store_b64(u32x2{__float_as_uint(sel4(y, g4)), tag}, rws, WS_Y + ((h * 4 + g4) * 640 + 80 * j + 16 * wave + n16) * 8, 0, AUX_SC1);
    }
  } else if (wave == 7 && h < 16) {
    const int fb = h & 3, fq = h >> 2;
    float gs = 0.f;
    {
      const rsrc_t rg = mk(p.gw, DM * 2);
#pragma unroll
      for (int i = 0; i < 5; ++i) {
        const unsigned xv = __builtin_amdgcn_raw_buffer_load_b32(rx, (fb * DM + 2 * lane + 128 * i) * 2, 0, 0);
        const unsigned gv = __builtin_amdgcn_raw_buffer_load_b32(rg, (2 * lane + 128 * i) * 2, 0, 0);
        gs += rbf(bflo(xv) * bflo(gv)) + rbf(bfhi(xv) * bfhi(gv));
      }
      gs = wave_sum(gs);
    }
    const float gate = rbf(1.f / (1.f + __expf(-rbf(gs))));
    const int hh = lane & 31, e2 = lane >> 5;
    float fs[10], as[10];
    {
      const int o = WS_YF + ((hh * 4 + fb) * 640 + 80 * j + 20 * fq + 10 * e2) * 8;
      for (unsigned spins = 0; spins < SPIN_CAP; ++spins) {
        asm volatile("" ::: "memory");
        bool ok = true;
#pragma unroll
        for (int i = 0; i < 5; ++i) {
          const u32x4 v = ld4<AUX_SC1>(rws, o + 16 * i, 0), v2 = ld4<AUX_SC1>(rws, o + 32 * (4 * 640 * 8) + 16 * i, 0);
          ok = ok && v[1] == tag && v[3] == tag && v2[1] == tag && v2[3] == tag;
          fs[2 * i] = __uint_as_float(v[0]) + __uint_as_float(v2[0]); fs[2 * i + 1] = __uint_as_float(v[2]) + __uint_as_float(v2[2]);
        }
        if (__all(ok)) break;
        __builtin_amdgcn_s_sleep(1);
      }
#pragma unroll
      for (int i = 0; i < 10; ++i) { float t = row16_sum(fs[i]); fs[i] = swap16_add(t, t); }
    }
    {
      const int o = WS_Y + ((hh * 4 + fb) * 640 + 80 * j + 20 * fq + 10 * e2) * 8;
      u32x4 v[5];
#pragma unroll
      for (int i = 0; i < 5; ++i) v[i] = ld4<AUX_SC1>(rws, o + 16 * i, 0);
      for (unsigned spins = 0; spins < SPIN_CAP; ++spins) {
        bool ok = true;
#pragma unroll
        for (int i = 0; i < 5; ++i) ok = ok & (v[i][1] == tag) & (v[i][3] == tag);
        if (__all(ok)) break;
        poll_pause();
        asm volatile("" ::: "memory");
        const int o2 = ok ? OOB : o;
#pragma unroll
        for (int i = 0; i < 5; ++i) { const u32x4 n = ld4<AUX_SC1>(rws, o2 + 16 * i, 0); v[i] = ok ? v[i] : n; }
      }
#pragma unroll
      for (int i = 0; i < 5; ++i) { as[2 * i] = __uint_as_float(v[i][0]); as[2 * i + 1] = __uint_as_float(v[i][2]); }
#pragma unroll
      for (int i = 0; i < 5; ++i) as[i] = row16_sum(swap16_add(as[i], as[i + 5]));
    }
    const int oi = 5 * ((lane >> 4) & 1) + n16;
    float av = as[0], fv = fs[0];
#pragma unroll
    for (int i = 1; i < 5; ++i) av = n16 == i ? as[i] : av;
#pragma unroll
    for (int i = 1; i < 10; ++i) fv = oi == i ? fs[i] : fv;
    const float res = rbf(gate * rbf(av)) + rbf(rbf(1.f - gate) * rbf(fv));
    if (n16 < 5) p.out[fb * DM + 80 * j + 20 * fq + 10 * e2 + oi] = (unsigned short)(cvtpk(res, res) & 0xffffu);
    if (w == 0 && lane == 0) __builtin_amdgcn_raw_buffer_store_b32(tag, rws, WS_E, 0, AUX_SC1);
  }
}

}

extern "C" void kernel_launch(void* const* d_in, const int* in_sizes, int n_in, void* d_out, int out_size, void* d_ws,
                              size_t ws_size, hipStream_t stream) {
  Params p;
  p.x = (const unsigned short*)d_in[0];
  p.kc = (const unsigned short*)d_in[1];
  p.vc = (const unsigned short*)d_in[2];
  p.wq = (const unsigned short*)d_in[3];
  p.wo = (const unsigned short*)d_in[4];
  p.w1 = (const unsigned short*)d_in[6];
  p.w3 = (const unsigned short*)d_in[7];
  p.w2 = (const unsigned short*)d_in[8];
  p.gw = (const unsigned short*)d_in[9];
  p.out = (unsigned short*)d_out;
  p.ws = (unsigned char*)d_ws;
  (void)hipFuncSetAttribute((const void*)fused_decode, hipFuncAttributeMaxDynamicSharedMemorySize, L_END);
  fused_decode<<<256, NTHR, L_END, stream>>>(p);
}
```
